```python
import torch

def _softmax(x, axis=-1):
    return torch.softmax(x, dim=axis)
def _gelu(x):
    return torch.nn.functional.gelu(x, approximate="tanh")
def _silu(x):
    return torch.nn.functional.silu(x)
def _sigmoid(x):
    return torch.sigmoid(x)
def _layernorm(x, axis=-1, eps=1e-5):
    m = x.mean(dim=axis, keepdim=True)
    v = x.var(dim=axis, keepdim=True, unbiased=False)
    return (x - m) / torch.sqrt(v + eps)
def _scatter_add(tgt, idx, src, axis=0):
    return tgt.clone().index_add_(axis, idx, src)
def _segment_sum(src, seg, n):
    out = torch.zeros((n,) + src.shape[1:], dtype=src.dtype)
    return out.index_add_(0, seg, src)
def _pad(x, pw):
    flat = tuple(v for pair in reversed(pw) for v in pair)
    return torch.nn.functional.pad(x, flat)

kernel_name = 'concat_heads__sliding_local_dilated__spectral_fnet__prompt'

def setup_inputs():

    _w = (lambda a: (lambda t: t.to(torch.bfloat16) if t.is_floating_point() else t)(torch.as_tensor(a)))
    def _A_setup():
        B, S, D = 1, 2048, 448
        return {
            "x": _w((lambda *s: torch.randn(*s, dtype=torch.bfloat16) * 0.2)(B, S, D)),
            "wqkv": _w((lambda *s: torch.randn(*s, dtype=torch.bfloat16) * 0.8)(D, 3 * D)),
            "wo": _w((lambda *s: torch.randn(*s, dtype=torch.bfloat16) * 0.2)(D, D)),
        }
    _A_in = _A_setup()

    def _B_setup():
        B, S, D = 1, 2048, 448
        ns = torch.arange(S)[:, None] * torch.arange(S)[None, :]
        nd = torch.arange(D)[:, None] * torch.arange(D)[None, :]
        Ws_re = torch.cos(-2.0 * torch.pi * ns / S).to(float)
        Ws_im = torch.sin(-2.0 * torch.pi * ns / S).to(float)
        Wd_re = torch.cos(-2.0 * torch.pi * nd / D).to(float)
        Wd_im = torch.sin(-2.0 * torch.pi * nd / D).to(float)
        return {
            "x": _w((lambda *s: torch.randn(*s, dtype=torch.bfloat16) * 0.05)(B, S, D)),
            "dft_seq_re": _w(Ws_re),
            "dft_seq_im": _w(Ws_im),
            "dft_hid_re": _w(Wd_re),
            "dft_hid_im": _w(Wd_im),
        }
    _B_in = _B_setup()

    return {
        "A_x": _A_in["x"],
        "A_wqkv": _A_in["wqkv"],
        "A_wo": _A_in["wo"],
        "B_x": _B_in["x"],
        "B_dft_seq_re": _B_in["dft_seq_re"],
        "B_dft_seq_im": _B_in["dft_seq_im"],
        "B_dft_hid_re": _B_in["dft_hid_re"],
        "B_dft_hid_im": _B_in["dft_hid_im"],
    }

def reference(A_x, A_wqkv, A_wo, B_x, B_dft_seq_re, B_dft_seq_im, B_dft_hid_re, B_dft_hid_im):
    def _A_ref(x, wqkv, wo):
        B, S, D = x.shape
        H = 16; Dh = D // H
        W, dil = 8, 2
        qkv = (lambda x, *d: x.permute(*d))((x @ wqkv).reshape(B, S, 3, H, Dh), 2, 0, 3, 1, 4)
        q, k, v = qkv[0], qkv[1], qkv[2]
        scale = Dh ** -0.5
        pos = torch.arange(S)
        off = torch.arange(W) * dil
        idx = pos[:, None] - off[None, :]
        mask = idx >= 0
        idx_c = torch.where(mask, idx, 0).reshape(-1)
        kg = k[:, :, idx_c, :].reshape(B, H, S, W, Dh)
        vg = v[:, :, idx_c, :].reshape(B, H, S, W, Dh)
        logits = torch.einsum("bhsd,bhswd->bhsw", q, kg) * scale
        logits = torch.where(mask[None, None, :, :], logits, -1e9)
        attn = _softmax(logits, axis=-1)
        o = torch.einsum("bhsw,bhswd->bhsd", attn, vg)
        return (lambda x, *d: x.permute(*d))(o, 0, 2, 1, 3).reshape(B, S, D) @ wo

    def _B_ref(x, dft_seq_re, dft_seq_im, dft_hid_re, dft_hid_im):
        a = torch.einsum("bsd,ed->bse", x, dft_hid_re)
        b = torch.einsum("bsd,ed->bse", x, dft_hid_im)
        y = torch.einsum("ts,bse->bte", dft_seq_re, a) - torch.einsum("ts,bse->bte", dft_seq_im, b)
        return y

    a = _A_ref(A_x, A_wqkv, A_wo)
    b = _B_ref(B_x, B_dft_seq_re, B_dft_seq_im, B_dft_hid_re, B_dft_hid_im)
    return torch.concatenate([a, b], axis=-1)
```

```cpp
#include <hip/hip_runtime.h>
#include <hip/hip_bf16.h>

typedef unsigned short u16;
typedef __attribute__((ext_vector_type(8))) short bf16x8;
typedef __attribute__((ext_vector_type(4))) short s16x4;
typedef __attribute__((ext_vector_type(4))) float f32x4;
typedef __attribute__((ext_vector_type(2))) __bf16 bf2;
typedef __attribute__((ext_vector_type(2))) unsigned u32x2;
typedef __attribute__((ext_vector_type(4))) unsigned u32x4;

#define LDS_AS __attribute__((address_space(3)))
#define GLB_AS __attribute__((address_space(1)))

constexpr int D = 448;

#define STAMP(kern, idx)
#define STAMPC(kern, idx)
#define QDECL
#define QT(i)
#define QOUT(base)

typedef __attribute__((ext_vector_type(2))) float f32x2;
__device__ __forceinline__ unsigned pk2(float a, float b) {
    f32x2 v = {a, b};
    return __builtin_bit_cast(unsigned, __builtin_convertvector(v, bf2));
}
__device__ __forceinline__ void glds16a(const void* sbase, unsigned voff, const char* l) {
    const unsigned la = (unsigned)(size_t)(LDS_AS const char*)l;
    asm volatile("s_mov_b32 m0, %2\n\ts_nop 0\n\tglobal_load_lds_dwordx4 %0, %1" ::"v"(voff), "s"(sbase), "s"(la) : "memory");
}
#define NTS " nt"
__device__ __forceinline__ void glds16n(const void* sbase, unsigned voff, const char* l) {
    const unsigned la = (unsigned)(size_t)(LDS_AS const char*)l;
    asm volatile("s_mov_b32 m0, %2\n\ts_nop 0\n\tglobal_load_lds_dwordx4 %0, %1" NTS ::"v"(voff), "s"(sbase), "s"(la) : "memory");
}
__device__ __forceinline__ float vmax(float a, float b) { return __builtin_elementwise_maximum(a, b); }
__device__ __forceinline__ float vmax3(float a, float b, float c) { return vmax(vmax(a, b), c); }
__device__ __forceinline__ bf16x8 lds_rd128(const char* p) { return *reinterpret_cast<const bf16x8*>(p); }
__device__ __forceinline__ s16x4 lds_tr(const char* p) {
    return __builtin_amdgcn_ds_read_tr16_b64_v4i16((LDS_AS s16x4*)p);
}
__device__ __forceinline__ bf16x8 cat(s16x4 a, s16x4 b) {
    bf16x8 r;
    r[0] = a[0]; r[1] = a[1]; r[2] = a[2]; r[3] = a[3];
    r[4] = b[0]; r[5] = b[1]; r[6] = b[2]; r[7] = b[3];
    return r;
}
#define MFMA(a, b, c) __builtin_amdgcn_mfma_f32_16x16x32_bf16((a), (b), (c), 0, 0, 0)
template <int N> __device__ __forceinline__ void wait_vm() { asm volatile("s_waitcnt vmcnt(%0)" ::"i"(N) : "memory"); }
#define SB() __builtin_amdgcn_sched_barrier(0)
template <int NP, int NM> __device__ __forceinline__ void ilv() {
#pragma unroll
    for (int i = 0; i < NP; ++i) {
        __builtin_amdgcn_sched_group_barrier(0x8, 1, 0);
        __builtin_amdgcn_sched_group_barrier(0x100, 1, 0);
    }
    if constexpr (NM > 0) __builtin_amdgcn_sched_group_barrier(0x8, NM, 0);
}
#define BAR()                                               \
    do {                                                    \
        asm volatile("s_waitcnt lgkmcnt(0)" ::: "memory");  \
        __builtin_amdgcn_s_barrier();                       \
        asm volatile("" ::: "memory");                      \
    } while (0)
#define BARL()                                              \
    do {                                                    \
        asm volatile("" ::: "memory");                      \
        __builtin_amdgcn_s_barrier();                       \
        asm volatile("" ::: "memory");                      \
    } while (0)

__device__ __forceinline__ int kperm(int r) { return (r & ~12) | ((r & 4) << 1) | ((r & 8) >> 1); }

constexpr int LDS_TOTAL = 163840;
constexpr int WS_O = 4 << 20, WS_PX = 8 << 20, WS_HS = 16 << 20;
#define CODEMAX 38400u
constexpr unsigned CODE_MAX = CODEMAX;

typedef unsigned long long u64;
struct Hs {
    const unsigned* base;
    unsigned off, snap;
};
constexpr int HS_O = 16 * 256, HS_X = 32 * 256;
constexpr u64 M_LO = 0xffffull, M_HI = 0xffff0000ull, M_OW = 0xfull << 48, M_OR = 1ull << 52, M_R14 = 0x3fffull << 16;
constexpr u64 M_XW = 0xfull << 32, M_XR = 0xf0ull << 32;
__device__ __forceinline__ unsigned hs_off(int lane, int b, int bx, int ro, int co) {
    const int bo = HS_O + ro * 256 - 48 * 4;
    return (unsigned)(lane * 4 + (lane < 32 ? b : lane < 48 ? bx - 32 * 4 : lane == 52 ? bo + 48 + co * 4 : bo));
}
template <int N> __device__ __forceinline__ unsigned hs_take(unsigned v) {
    unsigned r;
    asm volatile("s_waitcnt vmcnt(%2)\n\tv_mov_b32 %0, %1" : "=v"(r) : "v"(v), "i"(N) : "memory");
    return r;
}
__device__ __forceinline__ void hs_rmw(const Hs& h, unsigned& v, u64 m, unsigned d = 0u) {
    asm volatile("s_mov_b64 exec, %4\n\tglobal_atomic_add %0, %1, %2, %3 sc0 sc1\n\ts_mov_b64 exec, -1" : "+v"(v) : "v"(h.off), "v"(d), "s"(h.base), "s"(m) : "memory");
}
__device__ __forceinline__ void hs_add(const Hs& h, u64 m) {
    const unsigned one = 1u;
    asm volatile("s_mov_b64 exec, %3\n\tglobal_atomic_add %0, %1, %2 sc1\n\ts_mov_b64 exec, -1" ::"v"(h.off), "v"(one), "s"(h.base), "s"(m) : "memory");
}
__device__ __forceinline__ bool hs_new(const Hs& h, unsigned v, u64 m) { return (__ballot(v == h.snap) & m) == 0; }
#define PSL 2
__device__ __forceinline__ void hs_wait(const Hs& h, u64 m, u64 e, unsigned d) {
#pragma unroll 1
    for (unsigned spin = 0; spin < 40000u; ++spin) {
        unsigned v = 0;
        hs_rmw(h, v, e, d);
        if (hs_new(h, hs_take<0>(v), m)) return;
        __builtin_amdgcn_s_sleep(PSL);
    }
}
#define PD2 8
__device__ __forceinline__ void hs_wait2(const Hs& h, u64 m, unsigned& a, unsigned& b) {
    unsigned n = 40000u;
    unsigned z = 0u;
    asm volatile("s_mov_b64 exec, %7\n"
                 "1:\n\tglobal_atomic_add %1, %3, %4, %5 sc0 sc1\n\ts_waitcnt vmcnt(1)\n\tv_cmp_eq_u32 vcc, %0, %6\n\ts_cbranch_vccz 2f\n\t"
                 "global_atomic_add %0, %3, %4, %5 sc0 sc1\n\ts_waitcnt vmcnt(1)\n\tv_cmp_eq_u32 vcc, %1, %6\n\ts_cbranch_vccz 2f\n\t"
                 "s_sub_u32 %2, %2, 1\n\ts_cbranch_scc0 1b\n"
                 "2:\n\ts_mov_b64 exec, -1"
                 : "+&v"(a), "+&v"(b), "+&s"(n) : "v"(h.off), "v"(z), "s"(h.base), "v"(h.snap), "s"(m) : "memory", "vcc", "scc");
}
__device__ __forceinline__ void hs_keep(unsigned a, unsigned b) { asm volatile("s_waitcnt vmcnt(0)" ::"v"(a), "v"(b) : "memory"); }
__device__ __forceinline__ bf16x8 ld16_p(__amdgpu_buffer_rsrc_t r, unsigned off) {
    return __builtin_bit_cast(bf16x8, __builtin_amdgcn_raw_buffer_load_b128(r, off, 0, 16));
}
__device__ __forceinline__ void st16_out(__amdgpu_buffer_rsrc_t r, unsigned off, u32x4 v) {
#define OPOL 16
    __builtin_amdgcn_raw_buffer_store_b128(v, r, off, 0, OPOL);
}
__device__ __forceinline__ void st16_sc1(__amdgpu_buffer_rsrc_t r, unsigned off, u32x4 v) {
    __builtin_amdgcn_raw_buffer_store_b128(v, r, off, 0, 16);
}
__device__ __forceinline__ void glds16m(const void* sbase, unsigned voff, const char* l, u64 mask) {
    const unsigned la = (unsigned)(size_t)(LDS_AS const char*)l;
    asm volatile("s_mov_b32 m0, %2\n\ts_mov_b64 exec, %3\n\tglobal_load_lds_dwordx4 %0, %1\n\ts_mov_b64 exec, -1" ::"v"(voff), "s"(sbase), "s"(la), "s"(mask) : "memory");
}
__device__ __forceinline__ void glds16c(const void* sbase, unsigned voff, const char* l) {
    const unsigned la = (unsigned)(size_t)(LDS_AS const char*)l;
    asm volatile("s_mov_b32 m0, %2\n\ts_nop 0\n\tglobal_load_lds_dwordx4 %0, %1 sc1" ::"v"(voff), "s"(sbase), "s"(la) : "memory");
}
__device__ __forceinline__ void glds16mc(const void* sbase, unsigned voff, const char* l, u64 mask) {
    const unsigned la = (unsigned)(size_t)(LDS_AS const char*)l;
    asm volatile("s_mov_b32 m0, %2\n\ts_mov_b64 exec, %3\n\tglobal_load_lds_dwordx4 %0, %1 sc1\n\ts_mov_b64 exec, -1" ::"v"(voff), "s"(sbase), "s"(la), "s"(mask) : "memory");
}
__device__ __forceinline__ void touch_p(const void* p, char* smem, int where) {
    const unsigned la = (unsigned)(size_t)(LDS_AS const char*)(smem + where);
    asm volatile("s_mov_b32 m0, %1\n\ts_nop 0\n\tglobal_load_lds_dword %0, off" ::"v"(p), "s"(la) : "memory");
}

__device__ __forceinline__ void code_touch(u64 pc0, int part, int lane, char* l) {
    const unsigned la = (unsigned)(size_t)(LDS_AS const char*)l;
    asm volatile("s_mov_b32 m0, %2\n\ts_nop 0\n\tglobal_load_lds_dword %0, %1" ::"v"(min((unsigned)(part * 48 + min(lane, 47)) * 128u, CODE_MAX)), "s"(pc0 & ~127ull), "s"(la) : "memory");
}

constexpr int A_SL = 16384, B_R0 = 6 * A_SL, B_SL = 20480, X_SL = 8192, H_SL = 10240, H_0 = B_R0 - H_SL, MISC = B_R0 + 3 * B_SL;
#define F2 0
#define F3 1
#define KP 5
#define ET 1
constexpr int CODE_PART = (CODE_MAX - 6144) / 7 / 128 * 128;
#define DK 2
constexpr int Y_ST = 5 * A_SL, Y_P = 336, O_P = 144;
constexpr int T_DUMMY = MISC, L_KEY = MISC + 1024, L_GO = 163328, W_0 = A_SL;
__device__ __forceinline__ unsigned wo_off(int lane, int c) {
    return (unsigned)((c * 32 + ((lane >> 5) & 1) * 8 + ((lane >> 3) & 3)) * 896 + ((lane & 7) ^ (((lane >> 4) & 3) << 1)) * 16);
}
#define SX 0, 4, 1, 1, 1, 0, 0, 0, 0, 0, 0
#define SC 0, 0, 0, 0, 0, 1, 1, 0, 0, 0, 0
#define W0X 1
#define NB15 2
#define PB2 13
#define DEF 1
#define BL16 1
#define WOL 0
#define RT1 0x420
#define RT2 0
#define DR 1
#define NTN 0
#define NTW 0
#define NTA ""
#define GVS 0
#define TCF " sc1"
#define TCH 0
#define RTC (TCH & 1 ? 8 + DK : 0)
#define TMK 0x94u
#define TEV 0x0b1807
#define WL 20
#define P3 9
#define T3O 4
#define T3D 0
#define TC0 0x76542310u
#define TC1 0x76530421u
#define TC2 0x43176502u
#define TB0 0x76543210u
#define TB1 0x76543210u
#define TB2 0x76543210u
#define PT 0x20110
#define TA0 0

#define TX 0
#define LP 2
#define DP 0
#define PRS 12
#define WP1 2
#define WP2 11
#define XP 3
#define TM -1
#define TW 3
#define VL 0
#define PRE 2
#define RCG 2
#define WE 0
#define XS 0
#define EPK 1
#define SP 2
#define PKB 6
constexpr int SCH_X[11] = {SX}, SCH_C[11] = {SC};
constexpr int sch_nc() { int n = 0; for (int i = 0; i < 11; ++i) n += SCH_C[i]; return n; }
constexpr int sch_ap() { int n = 0; for (int i = PB2 - 3; i < 11; ++i) n += 4 * SCH_C[i]; return n; }
constexpr int C2L = 3 - sch_nc();
static_assert(C2L == 0 || C2L == 1, "");
constexpr int sch_wait(int b, bool w0) {
    int n = b == 5 ? 5 : 0, m = 0;
    bool past = b < 7;
    for (int t = 4; t < b; ++t) {
        if (w0 && past) n += t == 8 + DK;
        for (int i = 0; i < SCH_X[t - 4]; ++i) { if (past) n += 2; if (m == b - 7) past = true; ++m; }
        if (past) n += 4 * SCH_C[t - 4];
        if (w0 && past && t == PB2) ++n;
        if (past && t == RTC) ++n;
    }
    return n;
}
template <int B> __device__ __forceinline__ void sch_w(int w) {
    if (sch_wait(B, true) != sch_wait(B, false) && w == 0) wait_vm<sch_wait(B, true)>();
    else wait_vm<sch_wait(B, false)>();
}
__device__ __forceinline__ void sch_wait_b(int b, int w) {
    switch (b) {
        case 5: sch_w<5>(w); break;
        case 6: sch_w<6>(w); break;
        case 7: sch_w<7>(w); break;
        case 8: sch_w<8>(w); break;
        case 9: sch_w<9>(w); break;
        case 10: sch_w<10>(w); break;
        case 11: sch_w<11>(w); break;
        case 12: sch_w<12>(w); break;
        case 13: sch_w<13>(w); break;
        default: break;
    }
}
struct BL {
    const char *xg, *hg;
    unsigned offX, offH[3];
    int kc, sx;
};
__device__ __forceinline__ void reqX(BL& q, char* smem, int w) {
    const char* gx = q.xg + q.kc * 128;
    const char* la = smem + q.sx * X_SL + w * 2048;
    glds16a(gx, q.offX, la);
    glds16a(gx + 8 * 896, q.offX, la + 1024);
    q.kc = q.kc == 6 ? 0 : q.kc + 1; q.sx = q.sx == 5 ? 0 : q.sx + 1;
}
__device__ __forceinline__ void reqH(BL& q, char* smem, int w, int k) {
    const char* gh = q.hg + q.kc * 128;
    const char* lh = smem + H_0 + k * H_SL + w * 2560;
    glds16a(gh, q.offH[0], lh);
    glds16a(gh, q.offH[1], lh + 1024);
    glds16m(gh, q.offH[2], lh + 2048, 0xffffffffull);
}
__device__ __forceinline__ void b_off(BL& q, int hn, int lane, int w) {
    const int r8 = lane >> 3, c = (lane & 7) ^ r8;
    q.offX = (unsigned)((w * 16 + r8) * 896 + c * 16);
#pragma unroll
    for (int i = 0; i < 3; ++i) q.offH[i] = (unsigned)(min(w * 20 + i * 8 + r8, hn) * 896 + (c ^ ((w & 1) * 4)) * 16);
    q.sx = 0;
}
__device__ __forceinline__ void b_load(char* smem, BL& q, const char* cg, const char* abg, u64 tb, int ks,
                                       bool narrow, const char* wog, bool duty, Hs& hs, int lane, int w, int pm) {
    const int r8 = lane >> 3, c = (lane & 7) ^ r8;
    unsigned offA[4];
#pragma unroll
    for (int i = 0; i < 4; ++i)
        offA[i] = (unsigned)(((w >> 1) * 64 + (((w & 1) * 2 + (i >> 1) + ks) & 3) * 16 + (i & 1) * 8 + r8) * 4096 + c * 16);
    const int rb0 = w * (narrow ? 32 : 40);
    const unsigned offB = (unsigned)((c >> 2) * 10240 + (rb0 + r8) * 64 + (c & 3) * 16);
    const u64 mB = narrow ? 1ull : ~0ull;
    int sa = 3;
    u64 tc = tb | 0x8888888800000000ull;
    const unsigned t3 = (unsigned)tb;
    auto reqC = [&]() {
        const char* g = cg + ((unsigned)tc & 15) * 128;
        const char* la = smem + sa * A_SL + w * 4096;
#pragma unroll
        for (int i = 0; i < 4; ++i) glds16a(g, offA[i], la + i * 1024);
        sa = sa == 5 ? 0 : sa + 1; tc >>= 4;
    };
    int sb = 0;
    tb = tb << 1 | 0x1111111100000000ull;
    auto reqB = [&]() {
        const char* g = abg + ((unsigned)tb & 15) * B_SL;
        const char* lb = smem + B_R0 + sb * B_SL + rb0 * 128;
#pragma unroll
        for (int i = 0; i < 4; ++i) glds16c(g + i * 512, offB, lb + i * 1024);
        glds16mc(g + 2048, offB, lb + 4096, mB);
        sb = sb == 2 ? 0 : sb + 1; tb >>= 4;
    };
    auto step = [&]() { reqB(); reqC(); };
    auto stw = [&]() { wait_vm<13>(); };
    unsigned hv = 0;
    const unsigned own = (lane & 15) == pm;
    const unsigned offT = (unsigned)((pm & ~1) * B_SL + (w >> 1) * 10240 + ((pm & 1) * 2 + (w & 1)) * (narrow ? 2048 : 2560) + min(lane, narrow ? 15 : 19) * 128);
    auto pull = [&](int p) {
        asm volatile("s_mov_b32 m0, %2\n\ts_nop 0\n\tglobal_load_lds_dword %0, %1" TCF ::"v"(offT), "s"(abg + p * B_SL), "i"(T_DUMMY) : "memory");
    };
    STAMP(0, 8);
#pragma unroll 1
    for (int k = 2; k < 7; ++k) {
        wait_vm<5>();
        BARL();
        reqH(q, smem, w, k); reqX(q, smem, w);
    }
    q.xg += 512 * 896;
#pragma unroll
    for (int i = 0; i < SCH_X[0]; ++i) reqX(q, smem, w);
#pragma unroll
    for (int i = 0; i < SCH_C[0]; ++i) reqC();
#pragma unroll
    for (int b = 5; b < 15; ++b) {
        sch_wait_b(b, w);
        BARL();
        if (b == 6) STAMP(0, 2);
        if (b == 7) STAMP(0, 14);
        if (b == 8 && w == 0) {
            const u32x2 t = *reinterpret_cast<const u32x2*>(smem + L_KEY + lane * 8);
            hs.off = t[0]; hs.snap = t[1];
        }
        if (b == 8 + DK) { STAMP(0, 3); if (w == 0) hs_add(hs, 1ull << pm); if (TCH & 1) pull(0); }
#pragma unroll
        for (int i = 0; i < SCH_X[b - 4]; ++i) reqX(q, smem, w);
#pragma unroll
        for (int i = 0; i < SCH_C[b - 4]; ++i) reqC();
        if (b == PB2 && w == 0) hs_rmw(hs, hv, M_LO);
    }
    {
        volatile LDS_AS unsigned* go = (volatile LDS_AS unsigned*)(smem + L_GO + 4);
        if (w == 0) {
            const u64 m3 = 3u << (t3 << 1 & 14) | 3u << (t3 >> 3 & 14) | 3u << (t3 >> 7 & 14);
            if (PB2 > 14 || !hs_new(hs, hs_take<sch_ap()>(hv), m3)) hs_wait(hs, m3, M_LO, own);
            *go = 1u;
        } else {
#pragma unroll 1
            for (unsigned spin = 0; spin < 400000u && *go == 0u; ++spin) if (!(GVS)) __builtin_amdgcn_s_sleep(1);
        }
        asm volatile("" ::: "memory");
    }
    STAMP(0, 4);
#define BARM() BARL()
#define FT(i)
    FT(0);
#define GV 0
#define G1 4
    reqB(); FT(1);
    if ((GV & 1) && w == 0) __builtin_amdgcn_s_sleep(G1);
    reqB(); FT(2);
    if ((GV & 8) && w == 0) __builtin_amdgcn_s_sleep(G1);
    if (!(GV & 2)) {
        if (w == 0) hs_rmw(hs, hv, M_LO);
        if (C2L) reqC();
    }
    FT(3);
    wait_vm<5 + ((GV & 2) ? 0 : 4 * C2L)>(); FT(4); BARM();
    FT(5);
    if (GV & 2) {
        if (w == 0) hs_rmw(hs, hv, M_LO);
        if (C2L) reqC();
    }
    if (DR == 0 && w == 0) hs_add(hs, 0x10001ull << pm | (duty ? M_OR : 0ull));
    step();
    {
        const unsigned v = hs_take<9 + 4 * C2L>(hv);
        if (w == 0) { if (!hs_new(hs, v, M_LO)) hs_wait(hs, M_LO, DR ? M_LO : M_LO | M_HI, own); }
    }
    BARM();
#pragma unroll 1
    for (int i = 1; i < 4; ++i) {
        if (DR && i == DR && w == 0) hs_add(hs, 0x10001ull << pm | (duty ? M_OR : 0ull));
        if ((TCH & 2) && i == DR) pull(1);
        step();
        stw(); BARM();
    }
    if (w == 0) hs_rmw(hs, hv, M_LO | M_HI, own);
    step();
    stw(); BARM();
    if (w == 0) { if (!hs_new(hs, hs_take<9>(hv), M_HI)) hs_wait(hs, M_HI, M_HI, 0u); }
#pragma unroll 1
    for (int i = 5; i < 13; ++i) {
        step();
        stw(); BARM();
    }
    const unsigned offW = wo_off(lane, w);
    reqB();
    if (duty) {
#pragma unroll
        for (int n = 0; n < 14; ++n) {
            if (n == WP1) { wait_vm<9 + WP1>(); BARM(); }
            if (n == WP2) { wait_vm<WP2>(); BARM(); }
            if (n < 12) glds16a(wog + ((n >> 2) * 128 + (n & 2) * 8 + (n & 1) * 4) * 896, offW, smem + W_0 + (n >> 2) * A_SL + w * 4096 + (n & 3) * 1024);
            else glds16a(wog + (384 + ((w >> 1) - w) * 32 + (w & 1) * 16 + (n & 1) * 4) * 896, offW, smem + W_0 + 3 * A_SL + w * 2048 + (n & 1) * 1024);
        }
    } else {
        wait_vm<9>(); BARM();
        wait_vm<0>(); BARM();
    }
    wait_vm<0>();
    BARM();
}
template <int NE, class F>
__device__ __forceinline__ void ab_mma(char* smem, unsigned dbase, unsigned sg, int lane, int sh, int ef, u16* ab, F post) {
    const int fr = lane & 15, fq = lane >> 4;
    const int z0 = fr * 128 + ((fq ^ (fr & 7)) * 16), z1 = z0 ^ 64;
    unsigned a0 = sh * 4096 + z0, a1 = sh * 4096 + z1, b0 = H_0 + ef * 2048 + z0, b1 = H_0 + ef * 2048 + z1;
    unsigned doff = dbase + (unsigned)(sh * 10240 + (ef * 16 + fr) * 64 + ((fq & 1) * 2 + (fq >> 1)) * 16);
    asm volatile("" : "+v"(a0), "+v"(a1), "+v"(b0), "+v"(b1), "+v"(doff));
    const char *sa0 = smem + a0, *sa1 = smem + a1, *sb0 = smem + b0, *sb1 = smem + b1;
    const __amdgpu_buffer_rsrc_t rs = __builtin_amdgcn_make_buffer_rsrc(ab, 0, 192 * B_SL, 0x00020000);
    f32x4 acc[NE][2];
    bf16x8 xf[2][2], hf[2][NE];
    auto rd = [&](int k2) {
        const char* sa = k2 ? sa1 : sa0;
        const char* sb = k2 ? sb1 : sb0;
#pragma unroll
        for (int ni = 0; ni < 2; ++ni) xf[k2][ni] = lds_rd128(sa + ni * 2048);
#pragma unroll
        for (int mi = 0; mi < NE; ++mi) hf[k2][mi] = lds_rd128(sb + mi * 2048);
    };
    auto mm = [&](int k2) {
#pragma unroll
        for (int mi = 0; mi < NE; ++mi)
#pragma unroll
            for (int ni = 0; ni < 2; ++ni) acc[mi][ni] = MFMA(xf[k2][ni], hf[k2][mi], acc[mi][ni]);
    };
    int rx = 0;
    auto adv = [&](int db) {
        const int da = rx == 5 ? -5 * X_SL : X_SL;
        rx = rx == 5 ? 0 : rx + 1;
        sa0 += da; sa1 += da; sb0 += db; sb1 += db;
    };
    constexpr int NRD = 2 + NE, NMF = 2 * NE;
#pragma unroll 1
    for (int p = 0; p < 2; ++p) {
#pragma unroll
        for (int a = 0; a < NE; ++a) {
            acc[a][0] = f32x4{0.f, 0.f, 0.f, 0.f}; acc[a][1] = f32x4{0.f, 0.f, 0.f, 0.f};
            asm volatile("" : "+v"(acc[a][0]), "+v"(acc[a][1]));
        }
        BAR();
        if (p == 0) STAMP(0, 1);
        rd(0);
        SB();
#pragma unroll 1
        for (int kt = 0; kt < 6; ++kt) {
            rd(1); mm(0);
            ilv<NRD, NMF - NRD>();
            SB();
            adv(H_SL);
            if (p) { if (kt == DK) wait_vm<0>(); }
            else if (kt == KP) post();
            BAR();
            rd(0); mm(1);
            ilv<NRD, NMF - NRD>();
            SB();
        }
        rd(1); mm(0);
        ilv<NRD, NMF - NRD>();
        SB();
        if (p) BAR();
        STAMP(0, 2 + 9 * p);
        mm(1);
        SB();
#pragma unroll
        for (int mi = 0; mi < NE; ++mi) {
            const f32x4 a = acc[mi][0], b = acc[mi][1];
            auto x = __builtin_amdgcn_permlane16_swap(pk2(a[0], a[1]) ^ sg, pk2(b[0], b[1]) ^ sg, false, false);
            auto y = __builtin_amdgcn_permlane16_swap(pk2(a[2], a[3]) ^ sg, pk2(b[2], b[3]) ^ sg, false, false);
            st16_sc1(rs, doff + mi * 1024u, u32x4{x[0], y[0], x[1], y[1]});
        }
        SB();
        STAMP(0, 9 + p);
        adv(-6 * H_SL);
        doff += B_SL;
    }
}
__device__ __forceinline__ float lo16(unsigned v) { return __uint_as_float(v << 16); }
__device__ __forceinline__ float hi16(unsigned v) { return __uint_as_float(v & 0xffff0000u); }
#define RSP 2
template <int NF>
__device__ __forceinline__ void y_mma(char* smem, int lane, int cw, u16* px, unsigned pxb, int ks, u16* out, unsigned ob0, Hs& hs) {
    const int fr = lane & 15, fq = lane >> 4;
    const int mh = cw >> 1, nh = cw & 1;
    f32x4 acc[5][4];
#pragma unroll
    for (int a = 0; a < NF; ++a)
#pragma unroll
        for (int b = 0; b < 4; ++b) { acc[a][b] = f32x4{0.f, 0.f, 0.f, 0.f}; asm volatile("" : "+v"(acc[a][b])); }
    const int z0 = fr * 128 + ((fq ^ (fr & 7)) * 16), z1 = z0 ^ 64;
    unsigned a0 = 3 * A_SL + mh * 8192 + z0, a1 = 3 * A_SL + mh * 8192 + z1, b0 = B_R0 + nh * (NF * 2048) + z0, b1 = B_R0 + nh * (NF * 2048) + z1;
    unsigned pl = pxb + (unsigned)(cw * 8192 + lane * 16), ph = pl + (unsigned)(6144 - 512 - lane * 8);
    if (NF == 5) asm volatile("" : "+v"(ph));
    const unsigned one = fq == (fr >> 2) ? 0x3f80u << ((fr & 1) * 16) : 0u, d0 = (fr & 2) ? 0u : one, d1 = (fr & 2) ? one : 0u;
    u32x4 s0 = {d0, d1, 0u, 0u}, s1 = {0u, 0u, d0, d1};
    unsigned own = lane == 32 + ks;
    asm volatile("" : "+v"(a0), "+v"(a1), "+v"(b0), "+v"(b1), "+v"(pl), "+v"(s0), "+v"(s1), "+v"(own));
    const char *sa0 = smem + a0, *sa1 = smem + a1, *sb0 = smem + b0, *sb1 = smem + b1;
    const __amdgpu_buffer_rsrc_t rs = __builtin_amdgcn_make_buffer_rsrc(px, 0, 48 * 4 * 32768, 0x00020000);
    const __amdgpu_buffer_rsrc_t rs_out = __builtin_amdgcn_make_buffer_rsrc(out, 0, 2048 * 896 * 2, 0x00020000);
    bf16x8 cf[2][4], xf[2][NF];
    constexpr int NRD = 4 + NF;
    auto rd1 = [&](int i, int set, int st) {
        if (i == 0) xf[set][0] = lds_rd128((set ? sb1 : sb0) + st * B_SL);
        else if (i < 5) cf[set][i - 1] = lds_rd128((set ? sa1 : sa0) + st * A_SL + (i - 1) * 2048);
        else xf[set][i - 4] = lds_rd128((set ? sb1 : sb0) + st * B_SL + (i - 4) * 2048);
    };
    int da = -3 * A_SL;
    auto half = [&](int set, int st, int tog, int lo = 0, int hi = 4) {
        int r = 0, n = 0;
#pragma unroll
        for (int i = 0; i < 4 * NF; ++i) {
            if ((i & 3) < lo || (i & 3) >= hi) continue;
            acc[i >> 2][i & 3] = MFMA(xf[set][i >> 2], cf[set][i & 3], acc[i >> 2][i & 3]);
            if (r < NRD && n == r * RSP / 2) { rd1(r, set ^ 1, st); ++r; }
            if (i == 4 * NF - 3 && tog == 1) sa0 += da;
            if (i == 4 * NF - 3 && tog == 2) { sa1 += da; da = -da; }
            ++n;
            SB();
        }
    };
    STAMP(0, 4);
    if (DR == 0) wait_vm<0>();
    BARL();
    QDECL; QT(0);
    STAMP(0, 5); STAMPC(0, 3);
#pragma unroll
    for (int i = 0; i < NRD; ++i) rd1(i, 0, 0);
    SB();
#pragma unroll 1
    for (int t = 0; t < (DEF == 2 ? 4 : 5); ++t) {
        half(0, 0, 0);
        if (DR == 1) wait_vm<0>();
        BAR();
        half(1, 1, 0); half(0, 1, 0);
        if (DR == 2) wait_vm<0>();
        BAR();
        half(1, 2, 0); half(0, 2, 1); BAR();
        half(1, 0, 2);
    }
    bf16x8 kf[4];
    unsigned ka = 0, kb = 0;
    hs_rmw(hs, kb, M_XR);
    half(0, 0, 0, 1, 3);
    if (!BL16) BAR();
    STAMP(0, 18); STAMPC(0, 4); QT(1);
    const int pw = ks * 32768;
    auto put = [&](int p, int h) {
        const int sl = 2 * (p - 1) + h;
        const f32x4 a = acc[h < 2 ? 2 * h : NF - 1][p], b = acc[h < 2 ? 2 * h + 1 : 0][p];
        if (h < 2) __builtin_amdgcn_raw_buffer_store_b128(u32x4{pk2(a[0], a[1]), pk2(a[2], a[3]), pk2(b[0], b[1]), pk2(b[2], b[3])}, rs, pl + (sl & 3) * 1024u, pw + (sl >> 2 << 12), 16);
        else __builtin_amdgcn_raw_buffer_store_b64(u32x2{pk2(a[0], a[1]), pk2(a[2], a[3])}, rs, ph + p * 512u, pw, 16);
    };
#pragma unroll
    for (int s = 0; s < 6; ++s) {
        const int set = s == 2 || s == 4 ? 0 : 1, l = s < 2 ? s + 1 : s < 4 ? 3 : 0, p = s == 1 ? 1 : s == 2 ? 2 : s == 4 ? 3 : 0;
#pragma unroll
        for (int mi = 0; mi < NF; ++mi) {
            acc[mi][l] = MFMA(xf[set][mi], cf[set][l], acc[mi][l]);
            SB();
            if (p > 0 && mi >= 1 && mi < 3) put(p, mi - 1);
            if (NF == 5 && p >= 1 && mi == 3) put(p, 2);
            if (DEF == 2 && s == 5) xf[0][mi] = lds_rd128(sb0 + B_SL + mi * 2048);
            SB();
        }
        if (BL16 == 1 && s == 4) BARL();
    }
#pragma unroll
    for (int mi = 0; mi < NF; ++mi) asm volatile("" : "+v"(acc[mi][0]));
    STAMP(0, 19); QT(2);
    const bool all = hs_new(hs, hs_take<0>(kb), M_XR);
    STAMP(0, 20); QT(3);
    hs_rmw(hs, ka, M_XW, own);
    int pr[4];
#pragma unroll
    for (int l = 1; l < 4; ++l) pr[l - 1] = ((ks - l) & 3) * 32768 + (l == 3 ? 4096 : 0);
    pr[3] = pr[2] - 4096;
    unsigned pl2 = pl, ph2 = ph;
    u32x4 s01 = s0 | s1;
    __amdgpu_buffer_rsrc_t ro2 = rs_out;
    asm volatile("" : "+s"(pr[0]), "+s"(pr[1]), "+s"(pr[2]), "+s"(pr[3]), "+s"(ro2));
    asm volatile("" : "+v"(pl2), "+v"(ph2), "+v"(s01));
    unsigned l2 = __builtin_amdgcn_mbcnt_hi(~0u, __builtin_amdgcn_mbcnt_lo(~0u, 0u));
    unsigned yw = Y_ST + mh * (16 * Y_P) + nh * (NF * 32) + (l2 & 15) * Y_P + (l2 >> 4) * 8;
    unsigned yr = Y_ST + mh * (16 * Y_P) + nh * (NF == 5 ? 192 : 128) + (l2 >> 3) * Y_P + (l2 & 7) * 16;
    unsigned ys = Y_ST + mh * (16 * Y_P) + 128 + (l2 >> 2) * Y_P + (l2 & 3) * 16;
    unsigned ob = ob0 + mh * (64 * 1792) + nh * (NF == 5 ? 192 : 128) + (l2 >> 3) * 1792 + (l2 & 7) * 16;
    unsigned os = ob0 + mh * (64 * 1792) + 128 + (l2 >> 2) * 1792 + (l2 & 3) * 16;
    asm volatile("" : "+v"(yw), "+v"(yr), "+v"(ob));
    unsigned yc = (unsigned)(size_t)(LDS_AS char*)(smem + L_GO + 8 + mh * 4);
    if (NF == 5) asm volatile("" : "+v"(ys), "+v"(os), "+v"(yc));
    auto fin = [&](int mi) {
        const f32x4 a = acc[mi][0];
        *reinterpret_cast<u32x2*>(smem + yw + mi * 32) = u32x2{pk2(a[0], a[1]), pk2(a[2], a[3])};
        SB();
    };
    __builtin_amdgcn_s_sleep(PD2);
    hs_wait2(hs, M_XW ^ 1ull << (32 + ks), ka, kb);
    STAMP(1, 0); QT(4);
    bf16x8 pv[2][3];
    u32x2 p5[3];
#pragma unroll
    for (int h = 0; h < (NF + 1) / 2; ++h)
#pragma unroll
        for (int l = 1; l < 4; ++l) {
            const int sl = 2 * (l - 1) + h;
            if (h < 2) pv[h][l - 1] = __builtin_bit_cast(bf16x8, __builtin_amdgcn_raw_buffer_load_b128(rs, pl2 + (sl & 3) * 1024u, pr[l - 1], 16));
            else p5[l - 1] = __builtin_amdgcn_raw_buffer_load_b64(rs, ph2 + l * 512u, pr[l == 3 ? 3 : l - 1], 16);
            SB();
        }
    if (!all) hs_add(hs, 1ull << (32 + ks));
#pragma unroll
    for (int h = 0; h < (NF + 1) / 2; ++h) {
        if (h < 2) {
#pragma unroll
            for (int l = 0; l < 3; ++l) {
                acc[2 * h][0] = MFMA(__builtin_bit_cast(bf16x8, s0), pv[h][l], acc[2 * h][0]);
                acc[2 * h + 1][0] = MFMA(__builtin_bit_cast(bf16x8, s1), pv[h][l], acc[2 * h + 1][0]);
                SB();
                if (h == 1 && l == 1) { fin(0); fin(1); }
            }
        } else {
            acc[4][0] = MFMA(__builtin_bit_cast(bf16x8, s01), __builtin_bit_cast(bf16x8, u32x4{p5[0][0], p5[0][1], p5[1][0], p5[1][1]}), acc[4][0]);
            acc[4][0] = MFMA(__builtin_bit_cast(bf16x8, s0), __builtin_bit_cast(bf16x8, u32x4{p5[2][0], p5[2][1], 0u, 0u}), acc[4][0]);
            SB();
        }
    }
    STAMP(1, 1); QT(5);
    asm volatile("" ::"v"(ka), "v"(kb));
    fin(2); fin(3);
    if (NF == 5) {
        fin(4);
        asm volatile("s_mov_b64 exec, 1\n\tds_add_rtn_u32 %0, %0, %1\n\ts_mov_b64 exec, -1" : "+v"(yc) : "v"(yc) : "memory");
    }
    asm volatile("" ::: "memory");
    const u32x4 r0 = *reinterpret_cast<const u32x4*>(smem + yr), r1 = *reinterpret_cast<const u32x4*>(smem + yr + 8 * Y_P);
    u32x4 r2;
    if (NF == 5) r2 = *reinterpret_cast<const u32x4*>(smem + ys);
    SB();
    __builtin_amdgcn_raw_buffer_store_b128(r0, ro2, ob, 0, OPOL);
    __builtin_amdgcn_raw_buffer_store_b128(r1, ro2, ob, 8 * 1792, OPOL);
    if (NF == 5) {
        unsigned second;
        asm volatile("v_readfirstlane_b32 %0, %1" : "=s"(second) : "v"(yc), "v"(r1[0]));
        if (second) __builtin_amdgcn_raw_buffer_store_b128(r2, ro2, os, 0, OPOL);
    }
}

constexpr int Q_WSL = 21504, Q_AR = 5 * Q_WSL, Q_ASL = 18432, QP = 784, Q_DUMMY = 162816, W_A = Q_DUMMY - 14 * 4096;
__device__ __forceinline__ void qkv_reqW(char* smem, const u16* __restrict__ wqkv, const unsigned (&offW)[6], int c, int kw, int sw) {
    const char* g = (const char*)wqkv + (long)kw * (32 * 1344 * 2);
    const char* sb = smem + sw * Q_WSL + c * 1024;
#pragma unroll
    for (int k = 0; k < 5; ++k) glds16a(g, offW[k], sb + k * 4096);
    if (c == 0) glds16a(g, offW[5], sb + 5 * 4096);
}
__device__ __forceinline__ void qkv_reqX(char* smem, const u16* __restrict__ xA, const unsigned (&offX)[5], int c, int kx, int sx) {
    const char* g = (const char*)xA + kx * 128;
    const char* sb = smem + Q_AR + sx * Q_ASL + c * 1024;
#pragma unroll
    for (int k = 0; k < 4; ++k) glds16a(g, offX[k], sb + k * 4096);
    if (c < 2) glds16a(g, offX[4], sb + 4 * 4096);
}
template <int NR, int NM, int HS, bool XT, class F, class G, class H>
__device__ __forceinline__ void qkv_wave(char* smem, const u16* __restrict__ xA, const u16* __restrict__ wqkv,
                                         int w, int lane, int G0, int R0, Hs& hs, int k0,
                                         const unsigned (&offW)[6], const unsigned (&offX)[5], unsigned& apad, F prep, G setup, H wreq) {
    const int fr = lane & 15, fq = lane >> 4, c = w & 3;
    const int u = fq & 1, ah = fq >> 1, q = (lane >> 2) & 3, p = lane & 3;
    constexpr int N0 = HS < NM ? 0 : 1;
    constexpr int NX = NR + (XT ? 1 : 0), NW = NM + (XT ? 1 : 0);
    constexpr int NRD = NX - N0 + 2 * NW, NMF = NR * NM - HS + (XT ? 1 : 0);
    constexpr bool LATE = NR == 4, W0 = HS == 6;
    auto st = [&](int i) { const int k = k0 + i; return k >= 7 ? k - 7 : k; };
    f32x4 acc[NM * NR + 1];
#pragma unroll
    for (int a = 0; a < NM * NR + 1; ++a) acc[a] = f32x4{0.f, 0.f, 0.f, 0.f};
    const char* pw = smem + (16 * ah + 4 * u + q) * 672 + 8 * p + G0 * 32;
    const char* px0 = smem + Q_AR + (R0 * 16 + fr) * 128 + ((fq ^ (fr & 7)) * 16);
    const char* px1 = smem + Q_AR + (R0 * 16 + fr) * 128 + (((4 + fq) ^ (fr & 7)) * 16);
    const int dwx = (20 - G0) * 32, dxx = c * 2048;
    bf16x8 xf[2][NX];
    s16x4 wl[2][NW], wh[2][NW];
    int cw = 0, cx = 0;
    auto rd1 = [&](int i, int odd) {
        const char* xa = odd ? px1 : px0;
        if (i < NX - N0) { const int ni = i + N0; xf[odd][ni] = lds_rd128(xa + (ni < NR ? ni * 2048 : dxx)); }
        else {
            const int k = i - (NX - N0), mi = k >> 1;
            const char* pp = pw + (mi < NM ? mi * 32 : dwx);
            if (k & 1) wh[odd][mi] = lds_tr(pp + 8 * 672); else wl[odd][mi] = lds_tr(pp);
        }
    };
    auto tick = [&](int set) {
        int i = 0;
#pragma unroll
        for (int a = 0; a < NM; ++a)
#pragma unroll
            for (int b = 0; b < NR; ++b)
                if (b > 0 || a >= HS) {
                    acc[a * NR + b] = MFMA(cat(wl[set][a], wh[set][a]), xf[set][b], acc[a * NR + b]);
                    if (i < NRD) rd1(i, set ^ 1);
                    ++i;
                    SB();
                }
        if (XT) acc[NM * NR] = MFMA(cat(wl[set][NW - 1], wh[set][NW - 1]), xf[set][NX - 1], acc[NM * NR]);
        SB();
    };
    auto advW = [&]() { pw += cw == 4 ? -4 * Q_WSL : Q_WSL; cw = cw == 4 ? 0 : cw + 1; };
    auto advX = [&]() { const int d = cx == 2 ? -2 * Q_ASL : Q_ASL; px0 += d; px1 += d; cx = cx == 2 ? 0 : cx + 1; };
    unsigned pco[NW];
    auto prep2 = [&]() {
        int l2 = lane;
        asm volatile("" : "+v"(l2));
        __builtin_assume(l2 >= 0 && l2 < 64);
        prep(l2);
        const unsigned f2 = l2 & 15, c4 = (l2 >> 4) * 4;
#pragma unroll
        for (int mi = 0; mi < NM; ++mi) {
            const unsigned col = (G0 + mi) * 16 + c4;
            pco[mi] = (R0 * 16 + f2) * QP + col * 2 + (col * 147u >> 12) * 8;
        }
        if (XT) pco[NW - 1] = ((5 + c) * 16 + f2) * QP + 11 * 64 + 24 + c4 * 2;
#pragma unroll
        for (int mi = 0; mi < NW; ++mi) asm volatile("" : "+v"(pco[mi]));
    };
#pragma unroll
    for (int a = 0; a < NM * NR + 1; ++a)
        if (a < NM * NR ? (a % NR > 0 || a / NR >= HS) : XT) asm volatile("" : "+v"(acc[a]));
    auto tch = [&](int pos) {
#pragma unroll
        for (int i = 0; i < (PT >> 4 * pos & 15); ++i) wreq(-2);
    };
    unsigned hv = 0;
    if (W0) { setup(); hs_rmw(hs, hv, M_R14 | M_OW); }
    if (LATE) { __builtin_amdgcn_s_setprio(LP); wait_vm<0>(); }
    else if (W0) wait_vm<7>();
    else wait_vm<5>();
    BAR();
    STAMP(0, 1); STAMPC(0, 3);
#pragma unroll
    for (int i = 0; i < NRD; ++i) rd1(i, 0);
    advW();
    if (LATE) { qkv_reqX(smem, xA, offX, c, st(1), 1); qkv_reqW(smem, wqkv, offW, c, 2 * st(1), 2); }
    else { qkv_reqW(smem, wqkv, offW, c, 2 * st(1) + 1, 3); wait_vm<W0 ? 6 : 5>(); }
    BARL();
    STAMP(0, 9);
    SB();
    int sa = 4, sb = 0, sx = 2;
#pragma unroll 1
    for (int s = 0; s < 6; ++s) {
        if (XP && LATE && s < 5) {
            if (XP == 3) qkv_reqW(smem, wqkv, offW, c, 2 * st(s + 2), sa);
            if (XP != 5) qkv_reqX(smem, xA, offX, c, st(s + 2), sx);
            if (XP >= 4) qkv_reqW(smem, wqkv, offW, c, 2 * st(s + 2), sa);
            if (TX >> s & 1) wreq(-2);
            SB();
        }
        if (DP && LATE) { __builtin_amdgcn_s_setprio(0); SB(); }
        tick(0);
        if (DP && LATE) { __builtin_amdgcn_s_setprio(LP); SB(); }
        advW(); advX();
        if (LATE && s < 5) { if (XP < 3) qkv_reqW(smem, wqkv, offW, c, 2 * st(s + 2), sa); if (!XP || XP == 5) qkv_reqX(smem, xA, offX, c, st(s + 2), sx); if (TW == 5) wreq(-5); }
        else if (LATE) { wait_vm<(PT >> 4 & 15)>(); tch(2); }
        else wait_vm<0>();
        if (LATE) {
            int s2 = s;
            asm volatile("" : "+s"(s2));
            if (s2 == SP) prep2();
            if (TX >> s2 & 1) wait_vm<10>();
            else wait_vm<TW == 5 ? 10 : 9>();
        }
        BAR();
        if (!LATE && s < 5) qkv_reqW(smem, wqkv, offW, c, 2 * st(s + 2) + 1, sb);
        if (!LATE) {
            int s2 = s;
            asm volatile("" : "+s"(s2));
            if (s2 == SP) prep2();
            if (W0 && s2 == 4) { hs.snap = hs_take<63>(hv); hs_add(hs, M_OR); }
            if (TW == 0 && s2 == 5) wreq(-1);
        }
        SB();
        tick(1);
        advW();
        if (LATE && (PT >> 4 & 15)) { int s3 = s; asm volatile("" : "+s"(s3)); if (s3 == 4) tch(1); }
        sa = sa >= 3 ? sa - 3 : sa + 2; sb = sb >= 3 ? sb - 3 : sb + 2; sx = sx == 2 ? 0 : sx + 1;
    }
    STAMP(0, 2); STAMPC(0, 5);
    if (TW == 1 || (TW == 0 && LATE)) wreq(-1);
#pragma unroll
    for (int i = 0; i < NRD; ++i) rd1(i, 1);
    BAR();
    STAMP(0, 7);
    if (LATE) tch(4);
    if (TW == 2) wreq(-1);
    if (LATE) tch(5);
    if (LATE) __builtin_amdgcn_s_setprio(0);
    char* pc[NW];
#pragma unroll
    for (int mi = 0; mi < NW; ++mi) pc[mi] = smem + pco[mi];
    char* pad = smem + apad;
#pragma unroll
    for (int f = 0; f < NMF + 2; ++f) {
#pragma unroll
        for (int e = 0; e < 2; ++e) {
            const int g = e ? f - 2 : f;
            if (g < 0 || g >= NMF) continue;
            int mi = NM, ni = NR, n = 0;
#pragma unroll
            for (int a = 0; a < NM; ++a)
#pragma unroll
                for (int b = 0; b < NR; ++b)
                    if (b > 0 || a >= HS) { if (n == g) { mi = a; ni = b; } ++n; }
            f32x4& d = acc[mi < NM ? mi * NR + ni : NM * NR];
            if (e == 0) {
                d = MFMA(cat(wl[0][mi], wh[0][mi]), xf[0][ni], d);
                d = MFMA(cat(wl[1][mi], wh[1][mi]), xf[1][ni], d);
            }
            else *reinterpret_cast<u32x2*>(pc[mi] + (mi < NM ? ni * 16 * QP : 0)) = u32x2{pk2(d[0], d[1]), pk2(d[2], d[3])};
        }
        if (f == 0) {
            *reinterpret_cast<u32x2*>(pad) = u32x2{0u, 0u};
            *reinterpret_cast<u32x2*>(pad + 64 * QP) = u32x2{0u, 0u};
            if (w < 2) *reinterpret_cast<u32x2*>(pad + 128 * QP) = u32x2{0u, 0u};
        }
        SB();
    }
}

__device__ __forceinline__ unsigned qkv_attn(char* smem, const u16* xA, const u16* wqkv, const char* wo, char* ws, int rt, int hg, int tid, int w,
                                         Hs& hs, int kst, int ro, int co, u64 cb, int qi, const char* woc) {
    const int lane = tid & 63, s0 = rt * 128, c = w & 3;
    unsigned offW[6], offX[5];
    if (w >= 4) {
#pragma unroll
        for (int k = 0; k < 5; ++k) {
            const int ix = min((c + 4 * k) * 64 + lane, 1151), row = ix >> 3, ps = ix & 7;
            offX[k] = (unsigned)(max(s0 - 16 + row, 0) * D + (ps ^ (row & 7)) * 8) * 2u;
            if (k < 4 || c < 2) glds16a((const char*)xA + kst * 128, offX[k], smem + Q_AR + c * 1024 + k * 4096);
        }
    }
#pragma unroll
    for (int k = 0; k < 6; ++k) {
        const int id = min((c + 4 * k) * 64 + lane, 1343), r = id / 42, pos = id - r * 42, seg = pos / 14, cc = pos - seg * 14;
        offW[k] = (unsigned)(kperm(r) * 1344 + seg * 448 + hg * 112 + cc * 8) * 2u;
        if (k == 0) { asm volatile("" : "+v"(offW[0])); asm volatile("" : "+s"(wqkv)); }
        if (w < 4 && (k < 5 || c == 0)) glds16a((const char*)wqkv + (long)(2 * kst) * (32 * 1344 * 2), offW[k], smem + c * 1024 + k * 4096);
    }
    u16* o = (u16*)(ws + WS_O);
    u64 tc;
    unsigned tk;
    int wd = 0;
    if (w < 4) qkv_reqW(smem, wqkv, offW, c, 2 * kst + 1, 1);
    else {
        tc = cb + (unsigned)((c * 32 + (lane >> 1)) * 4096);
        tk = (unsigned)(qi + TA0 + (lane & 1)) * 128u;
        asm volatile("" : "+v"(tc), "+v"(tk));
        if (TW == 5) { wd = (qi * 392 + 392 - 20 * WL + c * WL + min(lane, WL - 1)) * 128; asm volatile("" : "+v"(wd)); }
    }
    auto setup = [&]() {
        int l2 = lane;
        asm volatile("" : "+v"(l2));
        __builtin_assume(l2 >= 0 && l2 < 64);
        hs.off = hs_off(l2, HS_O + rt * 256, 0, ro, co);
    };
    const int fq = lane >> 4;
    const int par = w & 1, qb = w >> 1;
    unsigned aq, ak[2], av[2], m0[2], m1[2], arp, og, apad, wt, wv;
    f32x4 c0, c1;
    auto prep = [&](unsigned ln) {
        const unsigned fr = ln & 15, f4 = ln >> 4, q = (ln >> 2) & 3, p = ln & 3;
        aq = (16 + 2 * (16 * qb + fr) + par) * QP + f4 * 16;
        ak[0] = aq - 16 * QP + 256;
        ak[1] = (2 * min(16 * qb + 16 + fr, 71u) + par) * QP + 256 + f4 * 16;
        av[0] = (2 * (16 * qb + (ln >> 2)) + par) * QP + 512 + p * 8;
        av[1] = (2 * min(16 * qb + 16 + (ln >> 2), 71u) + par) * QP + 512 + p * 8;
        const int out = (int)(~((0x1feu << fr) & (s0 + qb == 0 ? ~0xffu : ~0u)) >> (4 * f4));
        unsigned b0[4], b1[4];
#pragma unroll
        for (int i = 0; i < 4; ++i) {
            b0[i] = (unsigned)__builtin_amdgcn_sbfe(out, i, 1); b1[i] = (unsigned)__builtin_amdgcn_sbfe(out, 16 + i, 1);
            c0[i] = __uint_as_float(b0[i] & 0xf149f2cau);
            c1[i] = __uint_as_float(b1[i] & 0xf149f2cau);
        }
#pragma unroll
        for (int i = 0; i < 2; ++i) {
            m0[i] = ~((b0[2 * i] & 0xffffu) | (b0[2 * i + 1] & 0xffff0000u));
            m1[i] = ~((b1[2 * i] & 0xffffu) | (b1[2 * i + 1] & 0xffff0000u));
        }
        arp = (16 + 2 * (16 * qb + ((ln >> 1) & 15)) + par) * QP + (ln >> 5) * 32 + (ln & 1) * 16;
        og = (unsigned)(rt * 28 + hg * 7 + (ln >> 5)) * 4096u + (unsigned)(32 * qb + 16 * par) * 32u + (ln & 31) * 16u;
        apad = (w * 8 + (ln >> 3)) * QP + (ln & 7) * 64 + 56;
        wt = ((qi * 8 + w) * 49 + min(ln, 48u)) * 128;
        wv = wo_off(ln, w >> 2) + (((w >> 1) & 1) * 16 + (w & 1) * 4) * 896;
        asm volatile("" : "+v"(aq), "+v"(ak[0]), "+v"(ak[1]), "+v"(av[0]), "+v"(av[1]), "+v"(c0), "+v"(c1), "+v"(m0[0]), "+v"(m0[1]), "+v"(m1[0]), "+v"(m1[1]),
                          "+v"(arp), "+v"(og), "+v"(apad), "+v"(wt), "+v"(wv));
    };
    auto wreq = [&](int t) {
        if (t == -5) {
            asm volatile("s_mov_b32 m0, %2\n\ts_nop 0\n\tglobal_load_lds_dword %0, %1" ::"v"(max(wd, 0)), "s"(wo), "i"(Q_DUMMY) : "memory");
            wd += WL * 4 * 128;
        } else if (t < -1) {
            asm volatile("s_mov_b32 m0, %1\n\ts_nop 0\n\tglobal_load_lds_dword %0, off" ::"v"(tc + (tk & 0x380u)), "i"(Q_DUMMY) : "memory");
            tk += 256u;
        }
        else if (t < 0) asm volatile("s_mov_b32 m0, %2\n\ts_nop 0\n\tglobal_load_lds_dword %0, %1" ::"v"(wt), "s"(wo), "i"(Q_DUMMY) : "memory");
        else glds16a(woc + t * (64 * 896), wv, smem + W_A + t * 8192 + w * 1024);
    };
    if (w == 0) qkv_wave<5, 6, 6, false>(smem, xA, wqkv, w, lane, 0, 0, hs, kst, offW, offX, apad, prep, setup, wreq);
    else if (w == 1) qkv_wave<5, 5, 1, false>(smem, xA, wqkv, w, lane, 6, 0, hs, kst, offW, offX, apad, prep, setup, wreq);
    else if (w < 4) qkv_wave<5, 5, 0, false>(smem, xA, wqkv, w, lane, 1 + 5 * w, 0, hs, kst, offW, offX, apad, prep, setup, wreq);
    else qkv_wave<4, 5, 0, true>(smem, xA, wqkv, w, lane, 5 * (w - 4), 5, hs, kst, offW, offX, apad, prep, setup, wreq);
    STAMP(0, 3); STAMPC(0, 4);
    if (w >= 4) {
#pragma unroll
        for (int i = 0; i < (PT >> 24 & 15); ++i) wreq(-2);
    }
    if (TW == 3) wreq(-1);
    unsigned hv2 = 0;
    int w2 = w;
    asm volatile("" : "+s"(w2));
    if (w2 == 0) hs_rmw(hs, hv2, M_R14);
    const char* qa = smem + aq;
    const char* ka[2] = {smem + ak[0], smem + ak[1]};
    const char* va[2] = {smem + av[0], smem + av[1]};
    const float scale = 0.18898223650461361f, log2e = 1.4426950408889634f;
    BAR();
    if (!PRE && w >= 4) {
#pragma unroll 1
        for (int t = 1; t < 13; ++t) glds16a(woc + t * (32 * 896), wv, smem + W_A + t * 4096 + w * 1024);
    }
#define FTA(i)
    FTA(0);
    bf16x8 qf[4], k0[4], k1[4], v0[4], v1[4];
#pragma unroll
    for (int hh = 0; hh < 4; ++hh) {
        qf[hh] = lds_rd128(qa + hh * 64);
        k0[hh] = lds_rd128(ka[0] + hh * 64); k1[hh] = lds_rd128(ka[1] + hh * 64);
    }
    auto rdv = [&](int h0, int h1) {
        SB();
#pragma unroll
        for (int hh = h0; hh < h1; ++hh) {
            v0[hh] = cat(lds_tr(va[0] + hh * 64), lds_tr(va[1] + hh * 64));
            v1[hh] = cat(lds_tr(va[0] + hh * 64 + 32), lds_tr(va[1] + hh * 64 + 32));
        }
        SB();
    };
    if (VL == 0) rdv(0, 4);
    SB();
    FTA(1);
    float l[2][4], mx[2];
#pragma unroll
    for (int hp = 0; hp < 2; ++hp) {
        const f32x4 a0 = MFMA(k0[2 * hp], qf[2 * hp], c0), a1 = MFMA(k1[2 * hp], qf[2 * hp], c1);
        const f32x4 b0 = MFMA(k0[2 * hp + 1], qf[2 * hp + 1], c0), b1 = MFMA(k1[2 * hp + 1], qf[2 * hp + 1], c1);
        if (VL == 1) rdv(2 * hp, 2 * hp + 2);
        if (VL == 2 && hp == 1) rdv(0, 4);
        float t[4];
#pragma unroll
        for (int i = 0; i < 4; ++i) {
            auto r = __builtin_amdgcn_permlane32_swap(__float_as_uint(vmax(a0[i], a1[i])), __float_as_uint(vmax(b0[i], b1[i])), false, false);
            t[i] = vmax(__uint_as_float(r[0]), __uint_as_float(r[1]));
        }
#pragma unroll
        for (int i = 0; i < 2; ++i) {
            unsigned a = pk2(t[2 * i], t[2 * i + 1]);
            a = pk2(__uint_as_float(a << 16) * scale, __uint_as_float(a & 0xffff0000u) * scale);
            l[hp][2 * i] = __uint_as_float(a << 16);
            l[hp][2 * i + 1] = __uint_as_float(a & 0xffff0000u);
        }
        mx[hp] = vmax(vmax3(l[hp][0], l[hp][1], l[hp][2]), l[hp][3]);
    }
    {
        auto r = __builtin_amdgcn_permlane16_swap(__float_as_uint(mx[0]), __float_as_uint(mx[1]), false, false);
        const float c = vmax(__uint_as_float(r[0]), __uint_as_float(r[1])) * -log2e;
        auto q2 = __builtin_amdgcn_permlane16_swap(__float_as_uint(c), __float_as_uint(c), false, false);
        mx[0] = __uint_as_float(q2[0]); mx[1] = __uint_as_float(q2[1]);
    }
    float sm[2];
#pragma unroll
    for (int hp = 0; hp < 2; ++hp) {
#pragma unroll
        for (int i = 0; i < 4; ++i) l[hp][i] = __builtin_amdgcn_exp2f(__builtin_fmaf(l[hp][i], log2e, mx[hp]));
        sm[hp] = (l[hp][0] + l[hp][1]) + (l[hp][2] + l[hp][3]);
    }
    {
        auto r = __builtin_amdgcn_permlane16_swap(__float_as_uint(sm[0]), __float_as_uint(sm[1]), false, false);
        const float c = __builtin_amdgcn_rcpf(__uint_as_float(r[0]) + __uint_as_float(r[1]));
        auto q2 = __builtin_amdgcn_permlane16_swap(__float_as_uint(c), __float_as_uint(c), false, false);
        sm[0] = __uint_as_float(q2[0]); sm[1] = __uint_as_float(q2[1]);
    }
    FTA(2);
    const f32x4 z = {0.f, 0.f, 0.f, 0.f};
    char* ow = smem + aq - fq * 8;
    u32x2 ob[4];
#pragma unroll
    for (int hp = 0; hp < 2; ++hp) {
        const unsigned p01 = pk2(l[hp][0] * sm[hp], l[hp][1] * sm[hp]), p23 = pk2(l[hp][2] * sm[hp], l[hp][3] * sm[hp]);
        auto e01 = __builtin_amdgcn_permlane32_swap(p01, p01, false, false);
        auto e23 = __builtin_amdgcn_permlane32_swap(p23, p23, false, false);
#pragma unroll
        for (int e = 0; e < 2; ++e) {
            const int hh = 2 * hp + e;
            u32x4 pw = {e01[e] & m0[0], e23[e] & m0[1], e01[e] & m1[0], e23[e] & m1[1]};
            const bf16x8 pf = __builtin_bit_cast(bf16x8, pw);
            const f32x4 o0 = MFMA(v0[hh], pf, z), o1 = MFMA(v1[hh], pf, z);
            *reinterpret_cast<u32x2*>(ow + hh * 56) = u32x2{pk2(o0[0], o0[1]), pk2(o0[2], o0[3])};
            ob[hh] = u32x2{pk2(o1[0], o1[1]), pk2(o1[2], o1[3])};
        }
    }
    if (fq < 3) {
#pragma unroll
        for (int hh = 0; hh < 4; ++hh) *reinterpret_cast<u32x2*>(ow + hh * 56 + 32) = ob[hh];
    }
    asm volatile("s_waitcnt lgkmcnt(0)" ::: "memory");
    FTA(3);
    {
        const char* rp = smem + arp;
        const __amdgpu_buffer_rsrc_t rs_o = __builtin_amdgcn_make_buffer_rsrc(o, 0, 2048 * 448 * 2, 0x00020000);
#pragma unroll
        for (int i = 0; i < 4; ++i)
            if (i < 3 || lane < 32) st16_sc1(rs_o, og + i * 8192u, *reinterpret_cast<const u32x4*>(rp + i * 64));
    }
    FTA(4);
    if (PRE == 2 ? w >= 4 : PRE && w < 4) {
        if (PRE == 3) __builtin_amdgcn_s_sleep(PRS);
#pragma unroll 1
        for (int t = PRE == 2 ? 1 : 2; t < (PRE == 2 ? 13 : 14); ++t) glds16a(woc + t * (32 * 896), wv, smem + W_A + t * 4096 + w * 1024);
    } else wait_vm<0>();
    const unsigned hv3 = hs_take<12>(hv2);
    FTA(5);
    BAR();
    wreq(0);
    return hv3;
}
__device__ __forceinline__ void oproj(char* smem, char* stg, int so, u16* __restrict__ o, u16* __restrict__ out, int rt, int rg, int ct, int lane) {
    const int fr = lane & 15, fq = lane >> 4;
    const __amdgpu_buffer_rsrc_t rs_o = __builtin_amdgcn_make_buffer_rsrc(o, 0, 2048 * 448 * 2, 0x00020000);
    const unsigned ga = (unsigned)(rt * 28 + (fq >> 1)) * 4096u + (unsigned)(rg * 16 + fr) * 32u + (unsigned)(fq & 1) * 16u;
    bf16x8 fa[14];
#pragma unroll
    for (int ks = 0; ks < 14; ++ks) fa[ks] = ld16_p(rs_o, ga + ks * 8192u);
    SB();
    const int u = fq & 1, ah = fq >> 1, q = (lane >> 2) & 3, p = lane & 3, s = 2 * u + (q >> 1);
    const char* aw[4];
#pragma unroll
    for (int c = 0; c < 4; ++c) aw[c] = smem + (16 * ah + 4 * u + q) * 128 + ((c ^ s) * 32) + 8 * p;
    unsigned sw = so + fr * O_P + fq * 8, sr = so + (lane >> 3) * O_P + (lane & 7) * 16;
    unsigned ob = (unsigned)((rt * 128 + (rg >> 1) * 32 + 2 * (lane >> 3) + (rg & 1)) * 1792 + ct * 128 + (lane & 7) * 16);
    asm volatile("" : "+v"(ob), "+v"(sw), "+v"(sr));
    f32x4 acc[4];
#pragma unroll
    for (int c = 0; c < 4; ++c) acc[c] = f32x4{0.f, 0.f, 0.f, 0.f};
    constexpr int OA = 9;
    bf16x8 fb[14][4];
#pragma unroll
    for (int ks = 0; ks < 14 + OA; ++ks) {
        if (ks >= OA) {
#pragma unroll
            for (int c = 0; c < 4; ++c) acc[c] = MFMA(fb[ks - OA][c], fa[ks - OA], acc[c]);
        }
        if (ks < 14) {
#pragma unroll
            for (int c = 0; c < 4; ++c) fb[ks][c] = cat(lds_tr(aw[c] + ks * 4096), lds_tr(aw[c] + ks * 4096 + 1024));
        }
        SB();
    }
    STAMP(1, 3);
    const __amdgpu_buffer_rsrc_t rs_out = __builtin_amdgcn_make_buffer_rsrc(out, 0, 2048 * 896 * 2, 0x00020000);
#pragma unroll
    for (int c = 0; c < 4; ++c) *reinterpret_cast<u32x2*>(stg + sw + c * 32) = u32x2{pk2(acc[c][0], acc[c][1]), pk2(acc[c][2], acc[c][3])};
    asm volatile("" ::: "memory");
    const u32x4 r0 = *reinterpret_cast<const u32x4*>(stg + sr), r1 = *reinterpret_cast<const u32x4*>(stg + sr + 8 * O_P);
    SB();
    __builtin_amdgcn_raw_buffer_store_b128(r0, rs_out, ob, 0, OPOL);
    __builtin_amdgcn_raw_buffer_store_b128(r1, rs_out, ob, 16 * 1792, OPOL);
}

__global__ __launch_bounds__(512) void kf(const u16* xA, const u16* wqkv, const char* xB, const char* Hr, const char* Hi, const char* Cr,
                                          const char* Ci, const char* wo, u16* out, char* ws) {
    __shared__ __attribute__((aligned(16))) char smem[LDS_TOTAL];
    int tid = threadIdx.x, bid = blockIdx.x;
    u64 pc0;
    {
        unsigned part, off;
        asm volatile("s_getpc_b64 %0\n\ts_bfe_u32 %1, %2, 0x30003\n\ts_mulk_i32 %1, %3" : "=&s"(pc0), "=&s"(part), "+s"(bid) : "i"(CODE_PART) : "scc");
        asm volatile("s_mov_b32 m0, %4\n\tv_mad_u32_u24 %0, %1, 12, %2\n\tglobal_load_lds_dword %0, %3" : "=&v"(off), "+v"(tid) : "s"(part), "s"(pc0), "i"(Q_DUMMY) : "memory");
        asm volatile("" : "+s"(bid));
    }
    __builtin_assume(tid >= 0 && tid < 512 && bid >= 0 && bid < 256);
    const int lane = tid & 63, xcd = bid & 7;
    int w = __builtin_amdgcn_readfirstlane(tid >> 6), j = bid >> 3;
    STAMP(0, 0);
    const int ks = xcd & 3, rl = j & 3, c6 = j >> 2, r8 = (xcd >> 2) * 4 + rl;
    BL q;
    if (j < 24 && w < 4) {
        unsigned ox = (unsigned)(((ks & 1) * 1024 + r8 * 64) * 896), oh = (unsigned)((c6 < 5 ? c6 * 80 : 384) * 896);
#define KR 1
        q.kc = c6 + KR * rl >= 7 ? c6 + KR * rl - 7 : c6 + KR * rl;
        b_off(q, c6 < 4 ? 79 : 63, lane, w);
        const char *p0 = xB, *p1 = Hr, *p2 = Hi;
        asm volatile("" : "+v"(q.offX), "+v"(q.offH[0]), "+v"(q.offH[1]), "+v"(q.offH[2]));
        asm volatile("" : "+s"(p0), "+s"(p1), "+s"(p2), "+s"(ox), "+s"(oh), "+s"(q.kc));
        q.xg = p0 + ox;
        q.hg = ((ks >> 1) ? p2 : p1) + oh;
        STAMP(0, 7);
        reqH(q, smem, w, 0); reqX(q, smem, w);
        reqH(q, smem, w, 1); reqX(q, smem, w);
        asm volatile("" ::"s"(xA), "s"(wqkv), "s"(Cr), "s"(Ci), "s"(wo), "s"(out), "s"(ws));
    }
    asm volatile("" : "+s"(w), "+s"(j));
    __builtin_assume(w >= 0 && w < 8 && j >= 0 && j < 32);
    if (w == 7) *reinterpret_cast<u32x4*>(smem + L_GO) = u32x4{0u, 0u, 0u, 0u};
    Hs hs;
    hs.base = (const unsigned*)(ws + WS_HS); hs.off = 0; hs.snap = 0;
    int ro = 0, rg = 0, ct = 0;
    bool duty = true;
    u64 again = 0;
    unsigned ka = 0, kb = 0;
    int wb = W_0;
    if (j < 24) {
        u16* ab = (u16*)ws;
        u16* px = (u16*)(ws + WS_PX);
        const int rbl = j & 7, cg = j >> 3, rb = (xcd >> 2) * 8 + rbl;
#define DU 1
        duty = DU ? (j & 19) != 3 : j >= 4;
        const int un = DU ? j - min(c6, 4) : j - 4, uc = un >= 14 ? un - 14 : un;
        ro = xcd * 2 + (un >= 14); ct = (uc >> 1) & 7; rg = (uc & 1) * 4 + (w & 3);
        const int g1 = ks * 3 + cg;
        if (w < 4) {
            const char* cgp = ((ks >> 1) ? Ci : Cr) + (long)rb * (128 * 4096) + (ks & 1) * 2048;
            b_load(smem, q, cgp, (const char*)ab + g1 * (16 * B_SL), (u64)(((cg == 0 ? TB0 : cg == 1 ? TB1 : TB2) + rbl * 0x11111111u) & 0x77777777u) << 32 |
#define OWN 0
                   (((cg == 0 ? TC0 : cg == 1 ? TC1 : TC2) + rbl * 0x11111111u) & 0x77777777u), ks, cg == 2,
                   wo + (duty ? ct : 0) * 128, duty, hs, lane, w, r8 * 2 + (c6 & 1));
        } else {
            const int cw = w - 4;
            asm volatile("" ::"s"(xB), "s"(Hr), "s"(Hi));
            hs.off = hs_off(lane, g1 * 256, HS_X + ((rb * 3 + cg) * 4 + cw) * 256, ro, uc & 15);
            unsigned v1 = 0;
            hs_rmw(hs, v1, w == 5 ? M_LO | M_HI | M_XW | M_XR | M_OW : M_XW | M_XR);
            STAMP(0, 7);
            auto post = [&]() {
                if (w == 5) *reinterpret_cast<u32x2*>(smem + L_KEY + lane * 8) = u32x2{hs.off, hs_take<0>(v1)};
            };
            const unsigned dbase = (unsigned)((g1 * 16 + r8 * 2) * B_SL + (c6 & 1) * (c6 < 4 ? 80 : 64) * 64);
            const unsigned sg = (ks >> 1) ? 0x80008000u : 0u;
            if (c6 < 4 && !(cw & 1)) ab_mma<3>(smem, dbase, sg, lane, cw >> 1, 0, ab, post);
            else ab_mma<2>(smem, dbase, sg, lane, cw >> 1, (cw & 1) * (c6 < 4 ? 3 : 2), ab, post);
            hs.snap = hs_take<63>(v1);
            hs_add(hs, 1ull << (36 + ks));
            const unsigned pxb = (unsigned)(rb * 3 + cg) * 131072u, ob0 = (unsigned)((rb * 128 + ks * 16) * 896 + 448 + cg * 160) * 2u;
            if (cg == 2) y_mma<4>(smem, lane, cw, px, pxb, ks, out, ob0, hs);
            else y_mma<5>(smem, lane, cw, px, pxb, ks, out, ob0, hs);
            duty = false;
        }
    } else {
        const int qi = j - 24;
#define AM 1
        const int hg = xcd & 3, rt = (xcd >> 2) * 8 + qi;
#define XT4 0x5401
        int kst = qi + (XT4 >> (4 * hg) & 15); kst = kst >= 7 ? kst - 7 : kst;
        ro = xcd * 2 + 1; ct = 3 + (qi >> 1); rg = (qi & 1) * 4 + (w & 3); duty = w < 4;
        const unsigned hv = qkv_attn(smem, xA, wqkv, wo, ws, rt, hg, tid, w, hs, kst, ro, 6 + qi,
                                     (u64)(((ks >> 1) ? Ci : Cr) + (long)rt * (128 * 4096) + (ks & 1) * 2048 + 1024), qi, wo + ct * 128);
        STAMP(0, 21);
        if (w == 0) {
            hs_rmw(hs, ka, 1ull << hg | M_OW, lane == hg);
            if (!hs_new(hs, hv, M_R14)) again = 1ull << hg;
            wait_vm<1>();
        } else wait_vm<0>();
        BARL();
        wb = W_A;
    }
    if (duty) {
        QDECL; QT(0);
        volatile LDS_AS unsigned* go = (volatile LDS_AS unsigned*)(smem + L_GO);
        if (w == 0) { if (j < 24) { hs_rmw(hs, ka, M_OW); __builtin_amdgcn_s_sleep(PD2); } hs_wait2(hs, M_OW, ka, kb); *go = 1u; }
        else {
#pragma unroll 1
            for (unsigned spin = 0; spin < 400000u && *go == 0u; ++spin) __builtin_amdgcn_s_sleep(1);
        }
        asm volatile("" ::: "memory");
        STAMP(1, 0); QT(1);
        if (w == 0) hs_add(hs, M_OR);
        oproj(smem + wb, smem, (w & 3) * (16 * O_P), (u16*)(ws + WS_O), out, ro, rg, ct, lane);
        QT(2);
        hs_keep(ka, kb);
        QT(3); QOUT(10);
        if (again) { hs_wait(hs, M_R14, M_R14, 0u); hs_add(hs, again); }
    }
    STAMP(1, 24);
}

extern "C" void kernel_launch(void* const* d_in, const int* in_sizes, int n_in, void* d_out, int out_size,
                              void* d_ws, size_t ws_size, hipStream_t stream) {
    kf<<<256, 512, 0, stream>>>((const u16*)d_in[0], (const u16*)d_in[1], (const char*)d_in[3], (const char*)d_in[6], (const char*)d_in[7],
                                (const char*)d_in[4], (const char*)d_in[5], (const char*)d_in[2], (u16*)d_out, (char*)d_ws);
}
```

```cpp
#include <hip/hip_runtime.h>
#include <hip/hip_bf16.h>

typedef unsigned short u16;
typedef __attribute__((ext_vector_type(8))) short bf16x8;
typedef __attribute__((ext_vector_type(4))) short s16x4;
typedef __attribute__((ext_vector_type(4))) float f32x4;
typedef __attribute__((ext_vector_type(2))) __bf16 bf2;
typedef __attribute__((ext_vector_type(2))) unsigned u32x2;
typedef __attribute__((ext_vector_type(4))) unsigned u32x4;

#define LDS_AS __attribute__((address_space(3)))
#define GLB_AS __attribute__((address_space(1)))

constexpr int D = 448;

#define STAMP(kern, idx)
#define STAMPC(kern, idx)
#define QDECL
#define QT(i)
#define QOUT(base)

typedef __attribute__((ext_vector_type(2))) float f32x2;
__device__ __forceinline__ unsigned pk2(float a, float b) {
    f32x2 v = {a, b};
    return __builtin_bit_cast(unsigned, __builtin_convertvector(v, bf2));
}
__device__ __forceinline__ void glds16a(const void* sbase, unsigned voff, const char* l) {
    const unsigned la = (unsigned)(size_t)(LDS_AS const char*)l;
    asm volatile("s_mov_b32 m0, %2\n\ts_nop 0\n\tglobal_load_lds_dwordx4 %0, %1" ::"v"(voff), "s"(sbase), "s"(la) : "memory");
}
#define NTS " nt"
__device__ __forceinline__ void glds16n(const void* sbase, unsigned voff, const char* l) {
    const unsigned la = (unsigned)(size_t)(LDS_AS const char*)l;
    asm volatile("s_mov_b32 m0, %2\n\ts_nop 0\n\tglobal_load_lds_dwordx4 %0, %1" NTS ::"v"(voff), "s"(sbase), "s"(la) : "memory");
}
__device__ __forceinline__ float vmax(float a, float b) { return __builtin_elementwise_maximum(a, b); }
__device__ __forceinline__ float vmax3(float a, float b, float c) { return vmax(vmax(a, b), c); }
__device__ __forceinline__ bf16x8 lds_rd128(const char* p) { return *reinterpret_cast<const bf16x8*>(p); }
__device__ __forceinline__ s16x4 lds_tr(const char* p) {
    return __builtin_amdgcn_ds_read_tr16_b64_v4i16((LDS_AS s16x4*)p);
}
__device__ __forceinline__ bf16x8 cat(s16x4 a, s16x4 b) {
    bf16x8 r;
    r[0] = a[0]; r[1] = a[1]; r[2] = a[2]; r[3] = a[3];
    r[4] = b[0]; r[5] = b[1]; r[6] = b[2]; r[7] = b[3];
    return r;
}
#define MFMA(a, b, c) __builtin_amdgcn_mfma_f32_16x16x32_bf16((a), (b), (c), 0, 0, 0)
template <int N> __device__ __forceinline__ void wait_vm() { asm volatile("s_waitcnt vmcnt(%0)" ::"i"(N) : "memory"); }
#define SB() __builtin_amdgcn_sched_barrier(0)
#define EN 1
#define END() do { if (EN) asm volatile("s_endpgm" ::: "memory"); } while (0)
template <int NP, int NM> __device__ __forceinline__ void ilv() {
#pragma unroll
    for (int i = 0; i < NP; ++i) {
        __builtin_amdgcn_sched_group_barrier(0x8, 1, 0);
        __builtin_amdgcn_sched_group_barrier(0x100, 1, 0);
    }
    if constexpr (NM > 0) __builtin_amdgcn_sched_group_barrier(0x8, NM, 0);
}
#define BAR()                                               \
    do {                                                    \
        asm volatile("s_waitcnt lgkmcnt(0)" ::: "memory");  \
        __builtin_amdgcn_s_barrier();                       \
        asm volatile("" ::: "memory");                      \
    } while (0)
#define BARL()                                              \
    do {                                                    \
        asm volatile("" ::: "memory");                      \
        __builtin_amdgcn_s_barrier();                       \
        asm volatile("" ::: "memory");                      \
    } while (0)

__device__ __forceinline__ int kperm(int r) { return (r & ~12) | ((r & 4) << 1) | ((r & 8) >> 1); }

constexpr int LDS_TOTAL = 163840;
constexpr int WS_O = 4 << 20, WS_PX = 8 << 20, WS_HS = 16 << 20;
#define CODEMAX 38400u
constexpr unsigned CODE_MAX = CODEMAX;

typedef unsigned long long u64;
struct Hs {
    const unsigned* base;
    unsigned off, snap;
};
constexpr int HS_O = 16 * 256, HS_X = 32 * 256;
constexpr u64 M_LO = 0xffffull, M_HI = 0xffff0000ull, M_OW = 0xffffull << 48, M_OR = 1ull << 47, M_R14 = 0x3fffull << 32;
constexpr u64 M_XW = 0xfull << 32, M_XR = 0xf0ull << 32, M_XO = 0xeull << 32;
__device__ __forceinline__ unsigned hs_off(int lane, int b, int bx, int ro, int co) {
    const int bo = HS_O + ro * 256;
    return (unsigned)(lane * 4 + (lane < 32 ? b : lane < 47 ? bx - 32 * 4 : lane == 47 ? bo + 128 - 47 * 4 + co * 4 : bo - 48 * 4 + (co & 1) * 64));
}
template <int N> __device__ __forceinline__ unsigned hs_take(unsigned v) {
    unsigned r;
    asm volatile("s_waitcnt vmcnt(%2)\n\tv_mov_b32 %0, %1" : "=v"(r) : "v"(v), "i"(N) : "memory");
    return r;
}
__device__ __forceinline__ void hs_rmw(const Hs& h, unsigned& v, u64 m, unsigned d = 0u) {
    asm volatile("s_mov_b64 exec, %4\n\tglobal_atomic_add %0, %1, %2, %3 sc0 sc1\n\ts_mov_b64 exec, -1" : "+v"(v) : "v"(h.off), "v"(d), "s"(h.base), "s"(m) : "memory");
}
__device__ __forceinline__ void hs_add(const Hs& h, u64 m) {
    const unsigned one = 1u;
    asm volatile("s_mov_b64 exec, %3\n\tglobal_atomic_add %0, %1, %2 sc1\n\ts_mov_b64 exec, -1" ::"v"(h.off), "v"(one), "s"(h.base), "s"(m) : "memory");
}
__device__ __forceinline__ bool hs_new(const Hs& h, unsigned v, u64 m) { return (__ballot(v == h.snap) & m) == 0; }
#define PSL 2
__device__ __forceinline__ void hs_wait(const Hs& h, u64 m, u64 e, unsigned d) {
#pragma unroll 1
    for (unsigned spin = 0; spin < 40000u; ++spin) {
        unsigned v = 0;
        hs_rmw(h, v, e, d);
        if (hs_new(h, hs_take<0>(v), m)) return;
        __builtin_amdgcn_s_sleep(PSL);
    }
}
#define PD2 8
__device__ __forceinline__ void hs_wait2(const Hs& h, u64 m, unsigned& a, unsigned& b) {
    unsigned n = 40000u;
    unsigned z = 0u;
    asm volatile("s_mov_b64 exec, %7\n"
                 "1:\n\tglobal_atomic_add %1, %3, %4, %5 sc0 sc1\n\ts_waitcnt vmcnt(1)\n\tv_cmp_eq_u32 vcc, %0, %6\n\ts_cbranch_vccz 2f\n\t"
                 "global_atomic_add %0, %3, %4, %5 sc0 sc1\n\ts_waitcnt vmcnt(1)\n\tv_cmp_eq_u32 vcc, %1, %6\n\ts_cbranch_vccz 2f\n\t"
                 "s_sub_u32 %2, %2, 1\n\ts_cbranch_scc0 1b\n"
                 "2:\n\ts_mov_b64 exec, -1"
                 : "+&v"(a), "+&v"(b), "+&s"(n) : "v"(h.off), "v"(z), "s"(h.base), "v"(h.snap), "s"(m) : "memory", "vcc", "scc");
}
__device__ __forceinline__ void hs_wait3(const Hs& h, u64 m, unsigned& a, unsigned& b) {
    unsigned n = 40000u;
    unsigned z = 0u;
    asm volatile("s_mov_b64 exec, %7\n\ts_branch 3f\n"
                 "1:\n\tglobal_atomic_add %1, %3, %4, %5 sc0 sc1\n\ts_waitcnt vmcnt(1)\n\tv_cmp_eq_u32 vcc, %0, %6\n\ts_cbranch_vccz 2f\n\t"
                 "global_atomic_add %0, %3, %4, %5 sc0 sc1\n"
                 "3:\n\ts_waitcnt vmcnt(1)\n\tv_cmp_eq_u32 vcc, %1, %6\n\ts_cbranch_vccz 2f\n\t"
                 "s_sub_u32 %2, %2, 1\n\ts_cbranch_scc0 1b\n"
                 "2:\n\ts_mov_b64 exec, -1"
                 : "+&v"(a), "+&v"(b), "+&s"(n) : "v"(h.off), "v"(z), "s"(h.base), "v"(h.snap), "s"(m) : "memory", "vcc", "scc");
}
template <int SL = 0>
__device__ __forceinline__ void lds_spin(volatile LDS_AS unsigned* p, unsigned t) {
    unsigned n = 1000000u, v;
    if (SL == 0)
        asm volatile("1:\n\tds_read_b32 %0, %2\n\ts_sub_u32 %1, %1, 1\n\ts_cbranch_scc1 2f\n\ts_waitcnt lgkmcnt(0)\n\tv_cmp_gt_u32 vcc, %3, %0\n\ts_cbranch_vccnz 1b\n"
                     "2:\n\ts_waitcnt lgkmcnt(0)" : "=&v"(v), "+s"(n) : "v"(p), "s"(t) : "memory", "vcc", "scc");
    else
        asm volatile("s_branch 3f\n1:\n\ts_sleep %4\n3:\n\tds_read_b32 %0, %2\n\ts_sub_u32 %1, %1, 1\n\ts_cbranch_scc1 2f\n\ts_waitcnt lgkmcnt(0)\n\tv_cmp_gt_u32 vcc, %3, %0\n\ts_cbranch_vccnz 1b\n"
                     "2:\n\ts_waitcnt lgkmcnt(0)" : "=&v"(v), "+s"(n) : "v"(p), "s"(t), "i"(SL) : "memory", "vcc", "scc");
}
__device__ __forceinline__ void lds_add(volatile LDS_AS unsigned* p) {
    const unsigned one = 1u;
    asm volatile("s_mov_b64 exec, 1\n\tds_add_u32 %0, %1\n\ts_mov_b64 exec, -1" ::"v"(p), "v"(one) : "memory");
}
__device__ __forceinline__ void hs_keep(unsigned a, unsigned b) { asm volatile("s_waitcnt vmcnt(0)" ::"v"(a), "v"(b) : "memory"); }
__device__ __forceinline__ bf16x8 ld16_p(__amdgpu_buffer_rsrc_t r, unsigned off) {
    return __builtin_bit_cast(bf16x8, __builtin_amdgcn_raw_buffer_load_b128(r, off, 0, 16));
}
__device__ __forceinline__ void st16_out(__amdgpu_buffer_rsrc_t r, unsigned off, u32x4 v) {
#define OPOL 16
    __builtin_amdgcn_raw_buffer_store_b128(v, r, off, 0, OPOL);
}
__device__ __forceinline__ void st16_sc1(__amdgpu_buffer_rsrc_t r, unsigned off, u32x4 v) {
    __builtin_amdgcn_raw_buffer_store_b128(v, r, off, 0, 16);
}
__device__ __forceinline__ void glds16m(const void* sbase, unsigned voff, const char* l, u64 mask) {
    const unsigned la = (unsigned)(size_t)(LDS_AS const char*)l;
    asm volatile("s_mov_b32 m0, %2\n\ts_mov_b64 exec, %3\n\tglobal_load_lds_dwordx4 %0, %1\n\ts_mov_b64 exec, -1" ::"v"(voff), "s"(sbase), "s"(la), "s"(mask) : "memory");
}
__device__ __forceinline__ void glds16c(const void* sbase, unsigned voff, const char* l) {
    const unsigned la = (unsigned)(size_t)(LDS_AS const char*)l;
    asm volatile("s_mov_b32 m0, %2\n\ts_nop 0\n\tglobal_load_lds_dwordx4 %0, %1 sc1" ::"v"(voff), "s"(sbase), "s"(la) : "memory");
}
__device__ __forceinline__ void glds16mc(const void* sbase, unsigned voff, const char* l, u64 mask) {
    const unsigned la = (unsigned)(size_t)(LDS_AS const char*)l;
    asm volatile("s_mov_b32 m0, %2\n\ts_mov_b64 exec, %3\n\tglobal_load_lds_dwordx4 %0, %1 sc1\n\ts_mov_b64 exec, -1" ::"v"(voff), "s"(sbase), "s"(la), "s"(mask) : "memory");
}
__device__ __forceinline__ void touch_p(const void* p, char* smem, int where) {
    const unsigned la = (unsigned)(size_t)(LDS_AS const char*)(smem + where);
    asm volatile("s_mov_b32 m0, %1\n\ts_nop 0\n\tglobal_load_lds_dword %0, off" ::"v"(p), "s"(la) : "memory");
}

__device__ __forceinline__ void code_touch(u64 pc0, int part, int lane, char* l) {
    const unsigned la = (unsigned)(size_t)(LDS_AS const char*)l;
    asm volatile("s_mov_b32 m0, %2\n\ts_nop 0\n\tglobal_load_lds_dword %0, %1" ::"v"(min((unsigned)(part * 48 + min(lane, 47)) * 128u, CODE_MAX)), "s"(pc0 & ~127ull), "s"(la) : "memory");
}

constexpr int A_SL = 16384, B_R0 = 6 * A_SL, B_SL = 20480, X_SL = 8192, H_SL = 10240, H_0 = B_R0 - H_SL, MISC = B_R0 + 3 * B_SL;
#define F2 0
#define F3 1
#define KP 5
#define ET 1
constexpr int CODE_PART = (CODE_MAX - 6144) / 7 / 128 * 128;
#define DK 2
constexpr int Y_ST = 5 * A_SL + 2048, Y_P = 336, O_ST = 4 * A_SL + 8192, O_P = 144;
constexpr int T_DUMMY = MISC, L_KEY = MISC + 1024, L_GO = 163328, W_0 = A_SL;
__device__ __forceinline__ unsigned wo_off(int lane, int r) {
    return (unsigned)((r + (lane >> 3)) * 896 + ((lane & 7) ^ (((lane >> 4) & 3) << 1)) * 16);
}
#define SX 0, 4, 1, 1, 1, 0, 0, 0, 0, 0, 0
#define SC 0, 0, 0, 0, 0, 1, 1, 0, 0, 0, 0
#define W0X 1
#define NB15 2
#define PB2 12
#define DEF 1
#define BL16 1
#define WOL 0
#define RT1 0x420
#define RT2 0
#define DR 1
#define NTN 0
#define NTW 0
#define NTA ""
#define GVS 0
#define TCF " sc1"
#define TCH 0
#define RTC (TCH & 1 ? 8 + DK : 0)
#define TMK 0x94u
#define TEV 0x0b1807
#define WL 20
#define P3 9
#define T3O 4
#define T3D 0
#define TC0 0x76542310u
#define TC1 0x76530421u
#define TC2 0x43176502u
#define TB0 0x76543210u
#define TB1 0x76543210u
#define TB2 0x76543210u
#define PT 0x20110
#define TA0 0

#define TF 0
#define T0 0
#define T0B 0
#define TX 0
#define LP 2
#define DP 0
#define PRS 12
#define WP1 2
#define EL 14
#define HB 99
#define FB 3
#define YP 0
#define WM 0
#define GT 0
#define GS 4
#define GSL 2
#define AD 0
#define SNK 0
#define XE 1
#define XSL "2"
#define LT 1
#define CWT 1
#define BARC() do { if (CWT) BARL(); else BAR(); } while (0)
#define EP 0
#define WP2 11
#define XP 3
#define TM -1
#define TW 3
#define VL 0
#define PRE 2
#define RCG 2
#define WE 0
#define XS 0
#define EPK 1
#define SP -1
#define PKB 6
constexpr int SCH_X[11] = {SX}, SCH_C[11] = {SC};
constexpr int sch_nc() { int n = 0; for (int i = 0; i < 11; ++i) n += SCH_C[i]; return n; }
constexpr int sch_ap() { int n = 0; for (int i = PB2 - 3; i < 11; ++i) n += 4 * SCH_C[i]; return n; }
constexpr int C2L = 3 - sch_nc();
static_assert(C2L == 0 || C2L == 1, "");
constexpr int sch_wait(int b, bool w0) {
    int n = b == 5 ? 5 : 0, m = 0;
    bool past = b < 7;
    for (int t = 4; t < b; ++t) {
        if (w0 && past) n += t == 8 + DK;
        for (int i = 0; i < SCH_X[t - 4]; ++i) { if (past) n += 2; if (m == b - 7) past = true; ++m; }
        if (past) n += 4 * SCH_C[t - 4];
        if (w0 && past && t == PB2) ++n;
        if (past && t == RTC) ++n;
    }
    return n;
}
template <int B> __device__ __forceinline__ void sch_w(int w) {
    if (sch_wait(B, true) != sch_wait(B, false) && w == 0) wait_vm<sch_wait(B, true)>();
    else wait_vm<sch_wait(B, false)>();
}
__device__ __forceinline__ void sch_wait_b(int b, int w) {
    switch (b) {
        case 5: sch_w<5>(w); break;
        case 6: sch_w<6>(w); break;
        case 7: sch_w<7>(w); break;
        case 8: sch_w<8>(w); break;
        case 9: sch_w<9>(w); break;
        case 10: sch_w<10>(w); break;
        case 11: sch_w<11>(w); break;
        case 12: sch_w<12>(w); break;
        case 13: sch_w<13>(w); break;
        default: break;
    }
}
#define RA 0x76543210u
#define PI 0
__device__ __forceinline__ unsigned early(unsigned t) { return PI ? (t & 0x11111111u) << 2 | (t >> 1 & 0x33333333u) : t; }
struct BL {
    const char *xg, *hg;
    unsigned offX, offH[3];
    int kc, sx;
};
__device__ __forceinline__ void reqX(BL& q, char* smem, int w) {
    const char* gx = q.xg + q.kc * 128;
    const char* la = smem + q.sx * X_SL + w * 2048;
    glds16a(gx, q.offX, la);
    glds16a(gx + 8 * 896, q.offX, la + 1024);
    q.kc = q.kc == 6 ? 0 : q.kc + 1; q.sx = q.sx == 5 ? 0 : q.sx + 1;
}
__device__ __forceinline__ void reqH(BL& q, char* smem, int w, int k) {
    const char* gh = q.hg + q.kc * 128;
    const char* lh = smem + H_0 + k * H_SL + w * 2560;
    glds16a(gh, q.offH[0], lh);
    glds16a(gh, q.offH[1], lh + 1024);
    glds16m(gh, q.offH[2], lh + 2048, 0xffffffffull);
}
__device__ __forceinline__ void b_off(BL& q, int hn, int lane, int w) {
    const int r8 = lane >> 3, c = (lane & 7) ^ r8;
    q.offX = (unsigned)((w * 16 + r8) * 896 + c * 16);
#pragma unroll
    for (int i = 0; i < 3; ++i) q.offH[i] = (unsigned)(min(w * 20 + i * 8 + r8, hn) * 896 + (c ^ ((w & 1) * 4)) * 16);
    q.sx = 0;
}
__device__ __forceinline__ void wo_req(char* smem, const char* wog, unsigned offW, int w, int n) {
    if (n < 12) glds16a(wog + ((n >> 2) * 128 + (n & 3) * 8) * 896, offW, smem + W_0 + (n >> 2) * A_SL + w * 4096 + (n & 3) * 1024);
    else glds16a(wog + (384 - 16 * w + (n & 1) * 8) * 896, offW, smem + W_0 + 3 * A_SL + w * 2048 + (n & 1) * 1024);
}
__device__ __forceinline__ void b_load(char* smem, BL& q, const char* cg, const char* abg, u64 tb, int ks,
                                       bool narrow, const char* wog, u64 mduty, Hs& hs, int lane, int w, int pm
#define QL(i)
                                       ) {
    const int r8 = lane >> 3, c = (lane & 7) ^ r8;
    unsigned offA[4];
#pragma unroll
    for (int i = 0; i < 4; ++i)
        offA[i] = (unsigned)(((w >> 1) * 64 + (((w & 1) * 2 + (i >> 1) + ks) & 3) * 16 + (i & 1) * 8 + r8) * 4096 + c * 16);
    const int rb0 = w * (narrow ? 32 : 40);
    const unsigned offB = (unsigned)((c >> 2) * 10240 + (rb0 + r8) * 64 + (c & 3) * 16);
    const u64 mB = narrow ? 1ull : ~0ull;
    int sa = 3;
    u64 tc = tb | 0x8888888800000000ull;
    const unsigned t3 = (unsigned)tb;
    auto reqC = [&]() {
        const char* g = cg + ((unsigned)tc & 15) * 128;
        const char* la = smem + sa * A_SL + w * 4096;
#pragma unroll
        for (int i = 0; i < 4; ++i) glds16a(g, offA[i], la + i * 1024);
        sa = sa == 5 ? 0 : sa + 1; tc >>= 4;
    };
    int sb = 0;
    tb = tb << 1 | 0x1111111100000000ull;
    auto reqB = [&]() {
        const char* g = abg + ((unsigned)tb & 15) * B_SL;
        const char* lb = smem + B_R0 + sb * B_SL + rb0 * 128;
#pragma unroll
        for (int i = 0; i < 4; ++i) glds16c(g + i * 512, offB, lb + i * 1024);
        glds16mc(g + 2048, offB, lb + 4096, mB);
        sb = sb == 2 ? 0 : sb + 1; tb >>= 4;
    };
    auto step = [&]() { reqB(); reqC(); };
    unsigned vA[4], vB[4], lc;
    const char* gc;
    auto prepC = [&](int h) {
        if (h == 0) { gc = cg - 3072 + ((unsigned)tc & 15) * 128; tc >>= 4; asm volatile("" : "+s"(gc), "+s"(tc)); }
        else { lc = (unsigned)(size_t)(LDS_AS const char*)(smem + sa * A_SL + w * 4096); sa = sa == 5 ? 0 : sa + 1; asm volatile("" : "+s"(lc), "+s"(sa)); }
    };
    auto turn0 = [&]() {
#pragma unroll
        for (int i = 0; i < 4; ++i) { vA[i] = offA[i] + (3 - i) * 1024; vB[i] = offB + (3 - i) * 512; asm volatile("" : "+v"(vA[i]), "+v"(vB[i])); }
        prepC(0); prepC(1);
    };
    auto turn = [&]() {
        asm volatile("s_mov_b32 m0, %2\n\ts_nop 0\n\tglobal_load_lds_dwordx4 %0, %1" ::"v"(vA[0]), "s"(gc), "s"(lc) : "memory");
        SB();
        const char* g = abg - 1536 + ((unsigned)tb & 15) * B_SL;
        tb >>= 4;
        asm volatile("" : "+s"(g), "+s"(tb));
        asm volatile("global_load_lds_dwordx4 %0, %1 offset:1024" ::"v"(vA[1]), "s"(gc) : "memory");
        SB();
        unsigned lb = (unsigned)(size_t)(LDS_AS const char*)(smem + B_R0 + sb * B_SL + rb0 * 128);
        sb = sb == 2 ? 0 : sb + 1;
        asm volatile("" : "+s"(lb), "+s"(sb));
        asm volatile("global_load_lds_dwordx4 %0, %1 offset:2048" ::"v"(vA[2]), "s"(gc) : "memory");
        SB();
        unsigned l5 = lb + 2048;
        asm volatile("" : "+s"(l5));
        asm volatile("global_load_lds_dwordx4 %0, %1 offset:3072\n\ts_mov_b32 m0, %4\n\ts_waitcnt vmcnt(13)\n\ts_barrier\n\tglobal_load_lds_dwordx4 %2, %3 sc1"
                     ::"v"(vA[3]), "s"(gc), "v"(vB[0]), "s"(g), "s"(lb) : "memory");
        SB();
        prepC(0);
        asm volatile("global_load_lds_dwordx4 %0, %1 offset:1024 sc1" ::"v"(vB[1]), "s"(g) : "memory");
        SB();
        prepC(1);
        asm volatile("global_load_lds_dwordx4 %0, %2 offset:2048 sc1\n\tglobal_load_lds_dwordx4 %1, %2 offset:3072 sc1\n\t"
                     "s_mov_b32 m0, %4\n\ts_mov_b64 exec, %5\n\tglobal_load_lds_dwordx4 %3, %2 offset:2048 sc1\n\ts_mov_b64 exec, -1"
                     ::"v"(vB[2]), "v"(vB[3]), "s"(g), "v"(vB[0]), "s"(l5), "s"(mB) : "memory");
        SB();
    };
    auto stw = [&]() { wait_vm<13>(); };
    unsigned hv = 0;
    const unsigned own = (lane & 15) == pm;
    const unsigned offT = (unsigned)((pm & ~1) * B_SL + (w >> 1) * 10240 + ((pm & 1) * 2 + (w & 1)) * (narrow ? 2048 : 2560) + min(lane, narrow ? 15 : 19) * 128);
    auto pull = [&](int p) {
        asm volatile("s_mov_b32 m0, %2\n\ts_nop 0\n\tglobal_load_lds_dword %0, %1" TCF ::"v"(offT), "s"(abg + p * B_SL), "i"(T_DUMMY) : "memory");
    };
    STAMP(0, 8);
#pragma unroll 1
    for (int k = 2; k < 7; ++k) {
        wait_vm<5>();
        BARL();
        reqH(q, smem, w, k); reqX(q, smem, w);
    }
    QL(0);
    q.xg += 512 * 896;
#pragma unroll
    for (int i = 0; i < SCH_X[0]; ++i) reqX(q, smem, w);
#pragma unroll
    for (int i = 0; i < SCH_C[0]; ++i) reqC();
#pragma unroll
    for (int b = 5; b < 15; ++b) {
        sch_wait_b(b, w);
        BARL();
        if (b == 6) QL(1);
        if (b == 7) QL(2);
        if (b == 8 + DK) QL(3);
        if (b == 13) QL(4);
        if (b == 14) QL(5);
        if (b == 6) STAMP(0, 2);
        if (b == 7) STAMP(0, 14);
        if (b == 8 && w == 0) {
            const u32x2 t = *reinterpret_cast<const u32x2*>(smem + L_KEY + lane * 8);
            hs.off = t[0]; hs.snap = t[1];
        }
        if (b == 8 + DK) { STAMP(0, 3); if (w == 0) hs_add(hs, 1ull << pm); if (TCH & 1) pull(0); }
#pragma unroll
        for (int i = 0; i < SCH_X[b - 4]; ++i) reqX(q, smem, w);
#pragma unroll
        for (int i = 0; i < SCH_C[b - 4]; ++i) reqC();
        if (b == PB2 && w == 0) hs_rmw(hs, hv, M_LO);
    }
    {
        volatile LDS_AS unsigned* go = (volatile LDS_AS unsigned*)(smem + L_GO + 4);
        if (w == 0) {
            const u64 m3 = 3u << (t3 << 1 & 14) | 3u << (t3 >> 3 & 14) | 3u << (t3 >> 7 & 14);
            if (PB2 > 14 || !hs_new(hs, hs_take<sch_ap()>(hv), m3)) hs_wait(hs, m3, M_LO, own);
            *go = 1u;
        } else {
#pragma unroll 1
            for (unsigned spin = 0; spin < 400000u && *go == 0u; ++spin) if (!(GVS)) __builtin_amdgcn_s_sleep(1);
        }
        asm volatile("" ::: "memory");
    }
    STAMP(0, 4);
#define BARM() BARL()
#define FT(i)
    QL(6);
#define GV 0
#define G1 4
    reqB(); FT(1);
    if ((GV & 1) && w == 0) __builtin_amdgcn_s_sleep(G1);
    reqB(); FT(2);
    if ((GV & 8) && w == 0) __builtin_amdgcn_s_sleep(G1);
    if (!(GV & 2)) {
        if (w == 0) hs_rmw(hs, hv, M_LO);
        if (C2L) reqC();
    }
    QL(7);
    wait_vm<5 + ((GV & 2) ? 0 : 4 * C2L)>(); FT(4); BARM();
    QL(8);
    if (GV & 2) {
        if (w == 0) hs_rmw(hs, hv, M_LO);
        if (C2L) reqC();
    }
    if (DR == 0 && w == 0) hs_add(hs, 0x10001ull << pm | mduty);
    step();
    {
        const unsigned v = hs_take<9 + 4 * C2L>(hv);
        if (w == 0) { if (!hs_new(hs, v, M_LO)) hs_wait(hs, M_LO, DR ? M_LO : M_LO | M_HI, own); }
    }
    BARM();
    QL(9);
    if (w == 0) hs_add(hs, 0x10001ull << pm | mduty);
    reqB();
    turn0();
#pragma unroll 1
    for (int i = 2; i < 5; ++i) turn();
    if (w == 0) hs_rmw(hs, hv, M_LO | M_HI, own);
    turn();
    if (w == 0) { if (!hs_new(hs, hs_take<9>(hv), M_HI)) hs_wait(hs, M_HI, M_HI, 0u); }
#pragma unroll 1
    for (int i = 6; i < 14; ++i) turn();
    const unsigned offW = wo_off(lane, w * 32);
    asm volatile("s_cmp_eq_u64 %0, 0\n\ts_cbranch_scc0 1f\n\ts_waitcnt vmcnt(9)\n\ts_barrier\n\ts_waitcnt vmcnt(0)\n\ts_endpgm\n1:" ::"s"(mduty) : "memory", "scc");
#pragma unroll
    for (int n = 0; n < WP2; ++n) {
        if (n == WP1) { wait_vm<9 + WP1>(); BARM(); }
        wo_req(smem, wog, offW, w, n);
    }
    wait_vm<WP2>(); BARM();
}
template <int NE, class F>
__device__ __forceinline__ void ab_mma(char* smem, unsigned dbase, unsigned sg, int lane, int sh, int ef, u16* ab, F post) {
    const int fr = lane & 15, fq = lane >> 4;
    const int z0 = fr * 128 + ((fq ^ (fr & 7)) * 16), z1 = z0 ^ 64;
    unsigned a0 = sh * 4096 + z0, a1 = sh * 4096 + z1, b0 = H_0 + ef * 2048 + z0, b1 = H_0 + ef * 2048 + z1;
    unsigned doff = dbase + (unsigned)(sh * 10240 + (ef * 16 + fr) * 64 + ((fq & 1) * 2 + (fq >> 1)) * 16);
    asm volatile("" : "+v"(a0), "+v"(a1), "+v"(b0), "+v"(b1), "+v"(doff));
    const char *sa0 = smem + a0, *sa1 = smem + a1, *sb0 = smem + b0, *sb1 = smem + b1;
    const __amdgpu_buffer_rsrc_t rs = __builtin_amdgcn_make_buffer_rsrc(ab, 0, 192 * B_SL, 0x00020000);
    f32x4 acc[NE][2];
    bf16x8 xf[2][2], hf[2][NE];
    auto rd = [&](int k2) {
        const char* sa = k2 ? sa1 : sa0;
        const char* sb = k2 ? sb1 : sb0;
#pragma unroll
        for (int ni = 0; ni < 2; ++ni) xf[k2][ni] = lds_rd128(sa + ni * 2048);
#pragma unroll
        for (int mi = 0; mi < NE; ++mi) hf[k2][mi] = lds_rd128(sb + mi * 2048);
    };
    auto mm = [&](int k2) {
#pragma unroll
        for (int mi = 0; mi < NE; ++mi)
#pragma unroll
            for (int ni = 0; ni < 2; ++ni) acc[mi][ni] = MFMA(xf[k2][ni], hf[k2][mi], acc[mi][ni]);
    };
    int rx = 0;
    auto adv = [&](int db) {
        const int da = rx == 5 ? -5 * X_SL : X_SL;
        rx = rx == 5 ? 0 : rx + 1;
        sa0 += da; sa1 += da; sb0 += db; sb1 += db;
    };
    constexpr int NRD = 2 + NE, NMF = 2 * NE;
#pragma unroll 1
    for (int p = 0; p < 2; ++p) {
#pragma unroll
        for (int a = 0; a < NE; ++a) {
            acc[a][0] = f32x4{0.f, 0.f, 0.f, 0.f}; acc[a][1] = f32x4{0.f, 0.f, 0.f, 0.f};
            asm volatile("" : "+v"(acc[a][0]), "+v"(acc[a][1]));
        }
        BAR();
        if (p == 0) STAMP(0, 1);
        rd(0);
        SB();
#pragma unroll 1
        for (int kt = 0; kt < 6; ++kt) {
            rd(1); mm(0);
            ilv<NRD, NMF - NRD>();
            SB();
            adv(H_SL);
            if (p) { if (kt == DK) wait_vm<0>(); }
            else if (kt == KP) post();
            BAR();
            rd(0); mm(1);
            ilv<NRD, NMF - NRD>();
            SB();
        }
        rd(1); mm(0);
        ilv<NRD, NMF - NRD>();
        SB();
        if (p) BAR();
        STAMP(0, 2 + 9 * p);
        mm(1);
        SB();
#pragma unroll
        for (int mi = 0; mi < NE; ++mi) {
            const f32x4 a = acc[mi][0], b = acc[mi][1];
            auto x = __builtin_amdgcn_permlane16_swap(pk2(a[0], a[1]) ^ sg, pk2(b[0], b[1]) ^ sg, false, false);
            auto y = __builtin_amdgcn_permlane16_swap(pk2(a[2], a[3]) ^ sg, pk2(b[2], b[3]) ^ sg, false, false);
            st16_sc1(rs, doff + mi * 1024u, u32x4{x[0], y[0], x[1], y[1]});
        }
        SB();
        STAMP(0, 9 + p);
        adv(-6 * H_SL);
        doff += B_SL;
    }
}
__device__ __forceinline__ float lo16(unsigned v) { return __uint_as_float(v << 16); }
__device__ __forceinline__ float hi16(unsigned v) { return __uint_as_float(v & 0xffff0000u); }
#define RGR 0
#define GS2 1
#define RSP 2
template <int NF>
__device__ __forceinline__ void y_mma(char* smem, int lane, int cw, u16* px, unsigned pxb, int ks, u16* out, unsigned ob0, Hs& hs) {
    const int fr = lane & 15, fq = lane >> 4;
    const int mh = cw >> 1, nh = cw & 1;
    f32x4 acc[5][4];
#pragma unroll
    for (int a = 0; a < NF; ++a)
#pragma unroll
        for (int b = 0; b < 4; ++b) { acc[a][b] = f32x4{0.f, 0.f, 0.f, 0.f}; asm volatile("" : "+v"(acc[a][b])); }
    const int z0 = fr * 128 + ((fq ^ (fr & 7)) * 16), z1 = z0 ^ 64;
    unsigned a0 = 3 * A_SL + mh * 8192 + z0, a1 = 3 * A_SL + mh * 8192 + z1, b0 = B_R0 + nh * (NF * 2048) + z0, b1 = B_R0 + nh * (NF * 2048) + z1;
    unsigned pl = pxb + (unsigned)(cw * 8192 + lane * 16), ph = pl - (unsigned)(lane * 8);
    if (NF == 5) asm volatile("" : "+v"(ph));
    const unsigned one = fq == (fr >> 2) ? 0x3f80u << ((fr & 1) * 16) : 0u, d0 = (fr & 2) ? 0u : one, d1 = (fr & 2) ? one : 0u;
    u32x4 s0 = {d0, d1, 0u, 0u}, s1 = {0u, 0u, d0, d1};
    asm volatile("" : "+v"(a0), "+v"(a1), "+v"(b0), "+v"(b1), "+v"(pl), "+v"(s0), "+v"(s1));
    const char *sa0 = smem + a0, *sa1 = smem + a1, *sb0 = smem + b0, *sb1 = smem + b1;
    const __amdgpu_buffer_rsrc_t rs = __builtin_amdgcn_make_buffer_rsrc(px, 0, 48 * 4 * 32768, 0x00020000);
    const __amdgpu_buffer_rsrc_t rs_out = __builtin_amdgcn_make_buffer_rsrc(out, 0, 2048 * 896 * 2, 0x00020000);
    bf16x8 cf[2][4], xf[2][NF];
    constexpr int NRD = 4 + NF;
    auto rd1 = [&](int i, int set, int st) {
        if (i == 0) xf[set][0] = lds_rd128((set ? sb1 : sb0) + st * B_SL);
        else if (i < 5) cf[set][i - 1] = lds_rd128((set ? sa1 : sa0) + st * A_SL + (i - 1) * 2048);
        else xf[set][i - 4] = lds_rd128((set ? sb1 : sb0) + st * B_SL + (i - 4) * 2048);
    };
    int da = -3 * A_SL;
    auto half = [&](int set, int st, int tog, int lo = 0, int hi = 4, bool dr = false) {
        int r = 0, n = 0;
#pragma unroll
        for (int i = 0; i < 4 * NF; ++i) {
            if ((i & 3) < lo || (i & 3) >= hi) continue;
            const int x = i >> 2, y = SNK && hi - lo == 4 && (x & 1) ? 3 - (i & 3) : i & 3;
            acc[x][y] = MFMA(xf[set][x], cf[set][y], acc[x][y]);
            if (CWT) SB();
            if (r < NRD && n == r * RSP / 2) { rd1(r, set ^ 1, st); ++r; }
            if (CWT && i == 4 * NF - 2 && hi - lo == 4) __builtin_amdgcn_s_waitcnt(0xc07f);
            if (i == 4 * NF - (CWT == 1 ? 3 : 1) && dr) { SB(); wait_vm<0>(); }
            if (i == 4 * NF - 3 && tog == 1) sa0 += da;
            if (i == 4 * NF - 3 && tog == 2) { sa1 += da; da = -da; }
            ++n;
            SB();
        }
    };
    STAMP(0, 4);
    if (DR == 0) wait_vm<0>();
    if (YP == 2) __builtin_amdgcn_s_setprio(3);
    BARL();
    QDECL; QT(0);
    STAMP(0, 5); STAMPC(0, 3);
#pragma unroll
    for (int i = 0; i < NRD; ++i) rd1(i, 0, 0);
    SB();
#pragma unroll 1
    for (int t = 0; t < (DEF == 2 ? 4 : 5); ++t) {
        half(0, 0, 0, 0, 4, true);
        BARC();
        half(1, 1, 0); half(0, 1, 0); BARC();
        half(1, 2, 0); half(0, 2, 1); BARC();
        half(1, 0, 2);
    }
    bf16x8 kf[4];
    unsigned ka, kb = 0;
    asm volatile("v_mov_b32 %0, %1" : "=v"(ka) : "v"(hs.snap));
    hs_rmw(hs, kb, M_XR);
    half(0, 0, 0, 1, 3);
    STAMP(0, 18); STAMPC(0, 4); QT(1);
    if (YP == 1) __builtin_amdgcn_s_setprio(3);
    const int pw = ks * 32768;
    auto put = [&](int p, int h) {
        const f32x4 a = acc[h < 2 ? 2 * h : NF - 1][p], b = acc[h < 2 ? 2 * h + 1 : 0][p];
        if (h < 2) __builtin_amdgcn_raw_buffer_store_b128(u32x4{pk2(a[0], a[1]), pk2(a[2], a[3]), pk2(b[0], b[1]), pk2(b[2], b[3])}, rs, pl + h * 1024u, pw + (p - 1) * 2560, 16);
        else __builtin_amdgcn_raw_buffer_store_b64(u32x2{pk2(a[0], a[1]), pk2(a[2], a[3])}, rs, ph + 2048u, pw + (p - 1) * 2560, 16);
    };
#pragma unroll
    for (int s = 0; s < 6; ++s) {
        const int set = s == 2 || s == 4 ? 0 : 1, l = s < 2 ? s + 1 : s < 4 ? 3 : 0, p = s == 1 ? 1 : s == 2 ? 2 : s == 4 ? 3 : 0;
#pragma unroll
        for (int mi = 0; mi < NF; ++mi) {
            acc[mi][l] = MFMA(xf[set][mi], cf[set][l], acc[mi][l]);
            SB();
            if (p > 0 && mi >= 1 && mi < 3) put(p, mi - 1);
            if (NF == 5 && p >= 1 && mi == 3) put(p, 2);
            if (DEF == 2 && s == 5) xf[0][mi] = lds_rd128(sb0 + B_SL + mi * 2048);
            SB();
        }
        if (s == 4 && XE) { hs_rmw(hs, ka, M_XO); SB(); }
        if (s == GS) { lds_add((volatile LDS_AS unsigned*)(smem + L_GO)); SB(); }
    }
#pragma unroll
    for (int mi = 0; mi < NF; ++mi) asm volatile("" : "+v"(acc[mi][0]));
    STAMP(0, 19); QT(2);
    int pr[3];
#pragma unroll
    for (int l = 1; l < 4; ++l) pr[l - 1] = ((ks - l) & 3) * 32768 + (l - 1) * 2560;
    u32x2 sd = {s0[0], s0[1]};
    __amdgpu_buffer_rsrc_t ro2 = rs_out;
    asm volatile("" : "+s"(pr[0]), "+s"(pr[1]), "+s"(pr[2]), "+s"(ro2));
    asm volatile("" : "+v"(sd));
    unsigned l2 = __builtin_amdgcn_mbcnt_hi(~0u, __builtin_amdgcn_mbcnt_lo(~0u, 0u));
    unsigned yw = Y_ST + mh * (16 * Y_P) + nh * (NF * 32) + (l2 & 15) * Y_P + (l2 >> 4) * 8;
    unsigned yr = Y_ST + mh * (16 * Y_P) + nh * (NF == 5 ? 192 : 128) + (l2 >> 3) * Y_P + (l2 & 7) * 16;
    unsigned ys = Y_ST + mh * (16 * Y_P) + 128 + (l2 >> 2) * Y_P + (l2 & 3) * 16;
    unsigned ob = ob0 + mh * (64 * 1792) + nh * (NF == 5 ? 192 : 128) + (l2 >> 3) * 1792 + (l2 & 7) * 16;
    unsigned os = ob0 + mh * (64 * 1792) + 128 + (l2 >> 2) * 1792 + (l2 & 3) * 16;
    asm volatile("" : "+v"(yw), "+v"(yr), "+v"(ob));
    unsigned yc = (unsigned)(size_t)(LDS_AS char*)(smem + L_GO + 8 + mh * 4);
    if (NF == 5) asm volatile("" : "+v"(ys), "+v"(os), "+v"(yc));
    const bool all = hs_new(hs, hs_take<XE>(kb), M_XR);
    STAMP(0, 20); QT(3);
    hs_add(hs, 1ull << 32);
    u32x4 pv[3][2];
    u32x2 p5[3];
    unsigned pend = 14u, n = 200000u, t;
    const unsigned z = 0u;
#define X_LD(l, a, b, f, p) "s_bitcmp0_b32 %16, " #l "\n\ts_cbranch_scc1 1" #l "f\n\tbuffer_load_dwordx4 %" #a ", %21, %27, %" #p " offen sc1\n\t" \
    "buffer_load_dwordx4 %" #b ", %21, %27, %" #p " offen offset:1024 sc1\n\t" X_L5(f, p) "1" #l ":\n\t"
#define X_AD(c, l, a, b, f) c "s_waitcnt vmcnt(0)\n2" #l ":\n\t" \
    "v_mfma_f32_16x16x32_bf16 %0, %23, %" #a ", %0\n\tv_mfma_f32_16x16x32_bf16 %1, %24, %" #a ", %1\n\t" \
    "v_mfma_f32_16x16x32_bf16 %2, %23, %" #b ", %2\n\tv_mfma_f32_16x16x32_bf16 %3, %24, %" #b ", %3\n\t" X_A5(f)
#define X_ALL(W1, W2) asm volatile( \
    "1:\n\ts_mov_b32 exec_lo, 0\n\ts_mov_b32 exec_hi, 14\n" \
    "2:\n\tv_cmp_ne_u32 vcc, %14, %20\n\ts_and_b32 %16, vcc_hi, %15\n\ts_cbranch_scc1 3f\n\t" \
    "global_atomic_add %14, %18, %19, %26 sc0 sc1\n\ts_waitcnt vmcnt(20)\n\ts_sleep " XSL "\n\ts_sub_u32 %17, %17, 1\n\ts_cbranch_scc0 2b\n\ts_mov_b32 %16, %15\n" \
    "3:\n\ts_mov_b64 exec, -1\n\t" X_LD(1, 5, 6, 11, 28) X_LD(2, 7, 8, 12, 29) X_LD(3, 9, 10, 13, 30) \
    "s_xor_b32 %15, %15, %16\n\ts_cbranch_scc1 1b\n\t" \
    "s_bcnt1_i32_b32 %15, %16\n\ts_cmp_eq_u32 %15, 1\n\ts_waitcnt vmcnt(" W2 ")\n\ts_cbranch_scc0 4f\n\ts_waitcnt vmcnt(" W1 ")\n4:\n\t" \
    X_AD("s_bitcmp0_b32 %16, 1\n\ts_cbranch_scc1 21f\n\t", 1, 5, 6, 11) X_AD("s_cmp_eq_u32 %16, 8\n\ts_cbranch_scc1 22f\n\t", 2, 7, 8, 12) X_AD("", 3, 9, 10, 13) "s_nop " XN \
    : "+v"(acc[0][0]), "+v"(acc[1][0]), "+v"(acc[2][0]), "+v"(acc[3][0]), "+v"(acc[NF - 1][NF == 5 ? 0 : 1]), "=&v"(pv[0][0]), "=&v"(pv[0][1]), "=&v"(pv[1][0]), "=&v"(pv[1][1]), \
      "=&v"(pv[2][0]), "=&v"(pv[2][1]), "=&v"(p5[0]), "=&v"(p5[1]), "=&v"(p5[2]), "+&v"(ka), "+&s"(pend), "=&s"(t), "+&s"(n) \
    : "v"(hs.off), "v"(z), "v"(hs.snap), "v"(pl), "v"(ph), "v"(s0), "v"(s1), "v"(sd), "s"(hs.base), "s"(rs), "s"(pr[0]), "s"(pr[1]), "s"(pr[2]) : "memory", "vcc", "scc")
    if (NF == 5) {
#define X_L5(f, p) "buffer_load_dwordx2 %" #f ", %22, %27, %" #p " offen offset:2048 sc1\n"
#define X_A5(f) "v_mfma_f32_16x16x16_bf16 %4, %25, %" #f ", %4\n\t"
#define XN "3"
        X_ALL("3", "6");
#undef X_L5
#undef X_A5
    } else {
#define X_L5(f, p) "\n"
#define X_A5(f) ""
#undef XN
#define XN "4"
        X_ALL("2", "4");
#undef X_L5
#undef X_A5
    }
    u32x2 res[NF];
#pragma unroll
    for (int mi = 0; mi <= NF; ++mi) {
        if (mi < NF) { res[mi] = u32x2{pk2(acc[mi][0][0], acc[mi][0][1]), pk2(acc[mi][0][2], acc[mi][0][3])}; asm volatile("" : "+v"(res[mi])); }
        SB();
        if (mi > 0) *reinterpret_cast<u32x2*>(smem + yw + (mi - 1) * 32) = res[mi - 1];
        SB();
    }
    STAMP(1, 1); QT(5);
    if (NF == 5) {
        asm volatile("s_mov_b64 exec, 1\n\tds_add_rtn_u32 %0, %0, %1\n\ts_mov_b64 exec, -1" : "+v"(yc) : "v"(yc) : "memory");
    }
    asm volatile("" ::: "memory");
    const u32x4 r0 = *reinterpret_cast<const u32x4*>(smem + yr), r1 = *reinterpret_cast<const u32x4*>(smem + yr + 8 * Y_P);
    u32x4 r2;
    if (NF == 5) r2 = *reinterpret_cast<const u32x4*>(smem + ys);
    SB();
    __builtin_amdgcn_raw_buffer_store_b128(r0, ro2, ob, 0, OPOL);
    __builtin_amdgcn_raw_buffer_store_b128(r1, ro2, ob, 8 * 1792, OPOL);
    if (NF == 5) {
        unsigned second;
        asm volatile("v_readfirstlane_b32 %0, %1" : "=s"(second) : "v"(yc), "v"(r1[0]));
        if (second) __builtin_amdgcn_raw_buffer_store_b128(r2, ro2, os, 0, OPOL);
    }
    if (!all) hs_add(hs, 1ull << 32);
    END();
}

constexpr int Q_WSL = 21504, Q_AR = 5 * Q_WSL, Q_ASL = 18432, QP = 784, Q_DUMMY = 162816, W_A = Q_DUMMY - 14 * 4096;
__device__ __forceinline__ void qkv_reqW(char* smem, const u16* __restrict__ wqkv, const unsigned (&offW)[6], int c, int kw, int sw) {
    const char* g = (const char*)wqkv + (long)kw * (32 * 1344 * 2);
    const char* sb = smem + sw * Q_WSL + c * 1024;
#pragma unroll
    for (int k = 0; k < 5; ++k) glds16a(g, offW[k], sb + k * 4096);
    if (c == 0) glds16a(g, offW[5], sb + 5 * 4096);
}
__device__ __forceinline__ void qkv_reqX(char* smem, const u16* __restrict__ xA, const unsigned (&offX)[5], int c, int kx, int sx) {
    const char* g = (const char*)xA + kx * 128;
    const char* sb = smem + Q_AR + sx * Q_ASL + c * 1024;
#pragma unroll
    for (int k = 0; k < 4; ++k) glds16a(g, offX[k], sb + k * 4096);
    if (c < 2) glds16a(g, offX[4], sb + 4 * 4096);
}
template <int NR, int NM, int HS, bool XT, class F, class G, class H>
__device__ __forceinline__ unsigned qkv_wave(char* smem, const u16* __restrict__ xA, const u16* __restrict__ wqkv,
                                         int w, int lane, int G0, int R0, Hs& hs, int k0,
                                         const unsigned (&offW)[6], const unsigned (&offX)[5], unsigned& apad, F prep, G setup, H wreq) {
    const int fr = lane & 15, fq = lane >> 4, c = w & 3;
    const int u = fq & 1, ah = fq >> 1, q = (lane >> 2) & 3, p = lane & 3;
    constexpr int N0 = HS < NM ? 0 : 1;
    constexpr int NX = NR + (XT ? 1 : 0), NW = NM + (XT ? 1 : 0);
    constexpr int NRD = NX - N0 + 2 * NW, NMF = NR * NM - HS + (XT ? 1 : 0);
    constexpr bool LATE = NR == 4, W0 = HS == 6;
    auto st = [&](int i) { const int k = k0 + i; return k >= 7 ? k - 7 : k; };
    f32x4 acc[NM * NR + 1];
#pragma unroll
    for (int a = 0; a < NM * NR + 1; ++a) acc[a] = f32x4{0.f, 0.f, 0.f, 0.f};
    const char* pw = smem + (16 * ah + 4 * u + q) * 672 + 8 * p + G0 * 32;
    const char* px0 = smem + Q_AR + (R0 * 16 + fr) * 128 + ((fq ^ (fr & 7)) * 16);
    const char* px1 = smem + Q_AR + (R0 * 16 + fr) * 128 + (((4 + fq) ^ (fr & 7)) * 16);
    const int dwx = (20 - G0) * 32, dxx = c * 2048;
    bf16x8 xf[2][NX];
    s16x4 wl[2][NW], wh[2][NW];
    int cw = 0, cx = 0;
    auto rd1 = [&](int i, int odd) {
        const char* xa = odd ? px1 : px0;
        if (i < NX - N0) { const int ni = i + N0; xf[odd][ni] = lds_rd128(xa + (ni < NR ? ni * 2048 : dxx)); }
        else {
            const int k = i - (NX - N0), mi = k >> 1;
            const char* pp = pw + (mi < NM ? mi * 32 : dwx);
            if (k & 1) wh[odd][mi] = lds_tr(pp + 8 * 672); else wl[odd][mi] = lds_tr(pp);
        }
    };
    auto tick = [&](int set) {
        int i = 0;
#pragma unroll
        for (int a = 0; a < NM; ++a)
#pragma unroll
            for (int b = 0; b < NR; ++b)
                if (b > 0 || a >= HS) {
                    acc[a * NR + b] = MFMA(cat(wl[set][a], wh[set][a]), xf[set][b], acc[a * NR + b]);
                    if (i < NRD) rd1(i, set ^ 1);
                    ++i;
                    SB();
                }
        if (XT) acc[NM * NR] = MFMA(cat(wl[set][NW - 1], wh[set][NW - 1]), xf[set][NX - 1], acc[NM * NR]);
        SB();
    };
    auto advW = [&]() { pw += cw == 4 ? -4 * Q_WSL : Q_WSL; cw = cw == 4 ? 0 : cw + 1; };
    auto advX = [&]() { const int d = cx == 2 ? -2 * Q_ASL : Q_ASL; px0 += d; px1 += d; cx = cx == 2 ? 0 : cx + 1; };
    unsigned pco[NW];
    auto prep2 = [&]() {
        int l2 = lane;
        asm volatile("" : "+v"(l2));
        __builtin_assume(l2 >= 0 && l2 < 64);
        prep(l2);
        const unsigned f2 = l2 & 15, c4 = (l2 >> 4) * 4;
#pragma unroll
        for (int mi = 0; mi < NM; ++mi) {
            const unsigned col = (G0 + mi) * 16 + c4;
            pco[mi] = (R0 * 16 + f2) * QP + col * 2 + (col * 147u >> 12) * 8;
        }
        if (XT) pco[NW - 1] = ((5 + c) * 16 + f2) * QP + 11 * 64 + 24 + c4 * 2;
#pragma unroll
        for (int mi = 0; mi < NW; ++mi) asm volatile("" : "+v"(pco[mi]));
    };
#pragma unroll
    for (int a = 0; a < NM * NR + 1; ++a)
        if (a < NM * NR ? (a % NR > 0 || a / NR >= HS) : XT) asm volatile("" : "+v"(acc[a]));
    auto tch = [&](int pos) {
#pragma unroll
        for (int i = 0; i < (PT >> 4 * pos & 15); ++i) wreq(-2);
    };
    unsigned hv = 0;
    if (W0) { setup(); hs_rmw(hs, hv, M_R14 | M_OW); }
    if (SP < 0) prep2();
    if (LATE) { if (T0 == 1) wreq(-7); tch(0); __builtin_amdgcn_s_setprio(LP); wait_vm<(PT & 15) + (T0 == 1)>(); }
    else if (W0) wait_vm<7>();
    else wait_vm<5>();
    BAR();
    STAMP(0, 1); STAMPC(0, 3);
#pragma unroll
    for (int i = 0; i < NRD; ++i) rd1(i, 0);
    advW();
    if (LATE) { qkv_reqX(smem, xA, offX, c, st(1), 1); qkv_reqW(smem, wqkv, offW, c, 2 * st(1), 2); tch(3); if (T0 == 2) wreq(-7); }
    else { qkv_reqW(smem, wqkv, offW, c, 2 * st(1) + 1, 3); wait_vm<W0 ? 6 : 5>(); }
    BARL();
    STAMP(0, 9);
    SB();
    int sa = 4, sb = 0, sx = 2;
#pragma unroll 1
    for (int s = 0; s < 6; ++s) {
        if (XP && LATE && s < 5) {
            if (XP == 3) qkv_reqW(smem, wqkv, offW, c, 2 * st(s + 2), sa);
            if (XP != 5) qkv_reqX(smem, xA, offX, c, st(s + 2), sx);
            if (XP >= 4) qkv_reqW(smem, wqkv, offW, c, 2 * st(s + 2), sa);
            if (TX >> s & 1) wreq(TF ? -6 : -2);
            SB();
        }
        if (DP && LATE) { __builtin_amdgcn_s_setprio(0); SB(); }
        tick(0);
        if (DP && LATE) { __builtin_amdgcn_s_setprio(LP); SB(); }
        advW(); advX();
        if (LATE && s < 5) { if (XP < 3) qkv_reqW(smem, wqkv, offW, c, 2 * st(s + 2), sa); if (!XP || XP == 5) qkv_reqX(smem, xA, offX, c, st(s + 2), sx); if (TW == 5) wreq(-5); }
        else if (LATE) { wait_vm<(PT >> 4 & 15)>(); tch(2); }
        else wait_vm<0>();
        if (LATE) {
            int s2 = s;
            asm volatile("" : "+s"(s2));
            if (SP >= 0 && s2 == SP) prep2();
            if (TX >> s2 & 1) wait_vm<10>();
            else wait_vm<TW == 5 ? 10 : 9>();
        }
        BAR();
        if (!LATE && s < 5) qkv_reqW(smem, wqkv, offW, c, 2 * st(s + 2) + 1, sb);
        if (!LATE) {
            int s2 = s;
            asm volatile("" : "+s"(s2));
            if (SP >= 0 && s2 == SP) prep2();
            if (W0 && s2 == 4) { hs.snap = hs_take<63>(hv); hs_add(hs, M_OR); }
            if (TW == 0 && s2 == 5) wreq(-1);
        }
        SB();
        tick(1);
        advW();
        if (LATE && (PT >> 4 & 15)) { int s3 = s; asm volatile("" : "+s"(s3)); if (s3 == 4) tch(1); }
        sa = sa >= 3 ? sa - 3 : sa + 2; sb = sb >= 3 ? sb - 3 : sb + 2; sx = sx == 2 ? 0 : sx + 1;
    }
    STAMP(0, 2); STAMPC(0, 5);
    if (W0) { hv = 0; hs_rmw(hs, hv, M_R14); }
    if (TW == 1 || (TW == 0 && LATE)) wreq(-1);
#pragma unroll
    for (int i = 0; i < NRD; ++i) rd1(i, 1);
    BAR();
    STAMP(0, 7); wreq(-16);
    if (LATE) tch(4);
    if (TW == 2) wreq(-1);
    if (LATE) tch(5);
    if (!LATE) __builtin_amdgcn_s_setprio(3);
    char* pc[NW];
#pragma unroll
    for (int mi = 0; mi < NW; ++mi) pc[mi] = smem + pco[mi];
    char* pad = smem + apad;
    auto pos = [&](int g, int& mi, int& ni) {
        int n = 0;
        mi = NM; ni = NR;
#pragma unroll
        for (int a = 0; a < NM; ++a)
#pragma unroll
            for (int b = 0; b < NR; ++b)
                if (b > 0 || a >= HS) { if (n == g) { mi = a; ni = b; } ++n; }
    };
#pragma unroll
    for (int f = 0; f < NMF + FB; ++f) {
        int mi, ni, mj, nj;
        pos(f, mi, ni); pos(f - FB, mj, nj);
        f32x4& d = acc[mi < NM ? mi * NR + ni : NM * NR];
        const f32x4 e = acc[mj < NM ? mj * NR + nj : NM * NR];
        u32x2 t;
        if (f < NMF) d = MFMA(cat(wl[0][mi], wh[0][mi]), xf[0][ni], d);
        SB();
        if (f >= FB) { t = u32x2{pk2(e[0], e[1]), pk2(e[2], e[3])}; asm volatile("" : "+v"(t)); }
        SB();
        if (f < NMF) d = MFMA(cat(wl[1][mi], wh[1][mi]), xf[1][ni], d);
        SB();
        if (f >= FB) *reinterpret_cast<u32x2*>(pc[mj] + (mj < NM ? nj * 16 * QP : 0)) = t;
        else if (f < 2 || w < 2) *reinterpret_cast<u32x2*>(pad + f * 32 * QP) = u32x2{0u, 0u};
        if (LATE && f == HB) BARL();
        SB();
    }
    wreq(-17);
    return W0 ? hs_take<0>(hv) : 0u;
}

__device__ __forceinline__ unsigned qkv_attn(char* smem, const u16* xA, const u16* wqkv, const char* wo, char* ws, int rt, int hg, int tid, int w,
                                         Hs& hs, int kst, int ro, int co, u64 cb, int qi, const char* woc) {
    const int lane = tid & 63, s0 = rt * 128, c = w & 3;
    unsigned offW[6], offX[5];
    if (w >= 4) {
#pragma unroll
        for (int k = 0; k < 5; ++k) {
            const int ix = min((c + 4 * k) * 64 + lane, 1151), row = ix >> 3, ps = ix & 7;
            offX[k] = (unsigned)(max(s0 - 16 + row, 0) * D + (ps ^ (row & 7)) * 8) * 2u;
            if (k < 4 || c < 2) glds16a((const char*)xA + kst * 128, offX[k], smem + Q_AR + c * 1024 + k * 4096);
        }
    }
#pragma unroll
    for (int k = 0; k < 6; ++k) {
        const int id = min((c + 4 * k) * 64 + lane, 1343), r = id / 42, pos = id - r * 42, seg = pos / 14, cc = pos - seg * 14;
        offW[k] = (unsigned)(kperm(r) * 1344 + seg * 448 + hg * 112 + cc * 8) * 2u;
        if (k == 0) { asm volatile("" : "+v"(offW[0])); asm volatile("" : "+s"(wqkv)); }
        if (w < 4 && (k < 5 || c == 0)) glds16a((const char*)wqkv + (long)(2 * kst) * (32 * 1344 * 2), offW[k], smem + c * 1024 + k * 4096);
    }
    u16* o = (u16*)(ws + WS_O);
    u64 tc;
    unsigned tk, tf = TF;
    int wd = 0;
    if (w < 4) qkv_reqW(smem, wqkv, offW, c, 2 * kst + 1, 1);
    else {
        tc = cb + (unsigned)((c * 32 + (lane >> 1)) * 4096);
        tk = (unsigned)(qi + TA0 + (lane & 1)) * 128u;
        asm volatile("" : "+v"(tc), "+v"(tk));
        if (TW == 5) { wd = (qi * 392 + 392 - 20 * WL + c * WL + min(lane, WL - 1)) * 128; asm volatile("" : "+v"(wd)); }
    }
    auto setup = [&]() {
        int l2 = lane;
        asm volatile("" : "+v"(l2));
        __builtin_assume(l2 >= 0 && l2 < 64);
        hs.off = hs_off(l2, HS_O + rt * 256, HS_O + rt * 256 + 128, ro, co);
    };
    const int fq = lane >> 4;
    const int par = w & 1, qb = w >> 1;
    unsigned aq, ak[2], av[2], az[2], m0[2], m1[2], og, apad, wt, wv;
    f32x4 c0, c1;
    auto prep = [&](unsigned ln) {
        const unsigned fr = ln & 15, f4 = ln >> 4, p = ln & 3, lim = w < 4 ? 39u : 71u;
        aq = (16 + 2 * (16 * qb + fr) + par) * QP + f4 * 16;
        ak[0] = aq - 16 * QP + 256;
        ak[1] = (2 * min(16 * qb + 16 + fr, lim) + par) * QP + 256 + f4 * 16;
        av[0] = (2 * (16 * qb + (ln >> 2)) + par) * QP + 512 + p * 8;
        av[1] = (2 * min(16 * qb + 16 + (ln >> 2), lim) + par) * QP + 512 + p * 8;
        const int out = (int)(~((0x1feu << fr) & (s0 + qb == 0 ? ~0xffu : ~0u)) >> (4 * f4));
        unsigned b0[4], b1[4];
#pragma unroll
        for (int i = 0; i < 4; ++i) {
            b0[i] = (unsigned)__builtin_amdgcn_sbfe(out, i, 1); b1[i] = (unsigned)__builtin_amdgcn_sbfe(out, 16 + i, 1);
            c0[i] = __uint_as_float(b0[i] & 0xf149f2cau);
            c1[i] = __uint_as_float(b1[i] & 0xf149f2cau);
        }
#pragma unroll
        for (int i = 0; i < 2; ++i) {
            m0[i] = ~((b0[2 * i] & 0xffffu) | (b0[2 * i + 1] & 0xffff0000u));
            m1[i] = ~((b1[2 * i] & 0xffffu) | (b1[2 * i + 1] & 0xffff0000u));
        }
        az[0] = av[0] + (p == 3 ? 32 - 256 : 32); az[1] = av[1] + (p == 3 ? 32 - 256 : 32);
        og = (unsigned)((rt * 8 + w) * 16 + hg * 4) * 1024u + ln * 16u;
        apad = ((w >> 2) * 80 + c * 8 + (ln >> 3)) * QP + (ln & 7) * 64 + 56;
        wt = ((WM ? rt * 4 + hg : qi * 8 + w) * 49 + min(ln, 48u)) * 128;
        wv = wo_off(ln, w * 8);
        asm volatile("" : "+v"(aq), "+v"(ak[0]), "+v"(ak[1]), "+v"(av[0]), "+v"(av[1]), "+v"(c0), "+v"(c1), "+v"(m0[0]), "+v"(m0[1]), "+v"(m1[0]), "+v"(m1[1]),
                          "+v"(az[0]), "+v"(az[1]), "+v"(og), "+v"(apad), "+v"(wt), "+v"(wv));
    };
#define FTA(i)
    auto wreq = [&](int t) {
        if (t <= -10) { FTA(-10 - t); }
        else if (t == -6) {
            asm volatile("s_mov_b32 m0, %1\n\ts_nop 0\n\tglobal_load_lds_dword %0, off" ::"v"(tc + (u64)(long)((int)(tf & 7u) * 128 - 1024)), "i"(Q_DUMMY) : "memory");
            tf >>= 4;
        }
        else if (t == -7) asm volatile("s_mov_b32 m0, %1\n\ts_nop 0\n\tglobal_load_lds_dword %0, off" ::"v"(tc - 1024 + ((tk + T0B * 128u) & 0x380u)), "i"(Q_DUMMY) : "memory");
        else if (t == -5) {
            asm volatile("s_mov_b32 m0, %2\n\ts_nop 0\n\tglobal_load_lds_dword %0, %1" ::"v"(max(wd, 0)), "s"(wo), "i"(Q_DUMMY) : "memory");
            wd += WL * 4 * 128;
        } else if (t < -1) {
            asm volatile("s_mov_b32 m0, %1\n\ts_nop 0\n\tglobal_load_lds_dword %0, off" ::"v"(tc + (tk & 0x380u)), "i"(Q_DUMMY) : "memory");
            tk += 256u;
        }
        else if (t < 0 && WM && w != WM) {}
        else if (t < 0) asm volatile("s_mov_b32 m0, %2\n\ts_nop 0\n\tglobal_load_lds_dword %0, %1" ::"v"(wt), "s"(wo), "i"(Q_DUMMY) : "memory");
        else glds16a(woc + t * (64 * 896), wv, smem + W_A + t * 8192 + w * 1024);
    };
    unsigned hv3 = 0;
    if (w == 0) hv3 = qkv_wave<5, 6, 6, false>(smem, xA, wqkv, w, lane, 0, 0, hs, kst, offW, offX, apad, prep, setup, wreq);
    else if (w == 1) qkv_wave<5, 5, 1, false>(smem, xA, wqkv, w, lane, 6, 0, hs, kst, offW, offX, apad, prep, setup, wreq);
    else if (w < 4) qkv_wave<5, 5, 0, false>(smem, xA, wqkv, w, lane, 1 + 5 * w, 0, hs, kst, offW, offX, apad, prep, setup, wreq);
    else qkv_wave<4, 5, 0, true>(smem, xA, wqkv, w, lane, 5 * (w - 4), 5, hs, kst, offW, offX, apad, prep, setup, wreq);
    STAMP(0, 3); STAMPC(0, 4);
    if (w >= 4) {
#pragma unroll
        for (int i = 0; i < (PT >> 24 & 15); ++i) wreq(-2);
    }
    FTA(8);
    if (TW == 3) wreq(-1);
    FTA(9);
    const char* qa = smem + aq;
    const char* ka[2] = {smem + ak[0], smem + ak[1]};
    const char* va[2] = {smem + av[0], smem + av[1]};
    const float scale = 0.18898223650461361f, log2e = 1.4426950408889634f;
    volatile LDS_AS unsigned* cnt = (volatile LDS_AS unsigned*)(smem + L_GO);
    lds_add(cnt + 5 + (w >> 2));
    if (w < 4) __builtin_amdgcn_s_setprio(EP);
    else lds_spin(cnt + 6, 4u);
    lds_spin(cnt + 5, 4u);
    if (TW == 4) wreq(-1);
    FTA(0);
    bf16x8 qf[4], k0[4], k1[4], v0[4], v1[4];
#pragma unroll
    for (int hh = 0; hh < 4; ++hh) {
        qf[hh] = lds_rd128(qa + hh * 64);
        k0[hh] = lds_rd128(ka[0] + hh * 64); k1[hh] = lds_rd128(ka[1] + hh * 64);
    }
    auto rdv = [&](int h0, int h1) {
        SB();
#pragma unroll
        for (int hh = h0; hh < h1; ++hh) {
            v0[hh] = cat(lds_tr(va[0] + hh * 64), lds_tr(va[1] + hh * 64));
            v1[hh] = cat(lds_tr(smem + az[0] + hh * 64), lds_tr(smem + az[1] + hh * 64));
        }
        SB();
    };
    if (VL == 0) rdv(0, 4);
    if (TW == 6) wreq(-1);
    SB();
    FTA(1);
    float l[2][4], mx[2];
#pragma unroll
    for (int hp = 0; hp < 2; ++hp) {
        const f32x4 a0 = MFMA(k0[2 * hp], qf[2 * hp], c0), a1 = MFMA(k1[2 * hp], qf[2 * hp], c1);
        const f32x4 b0 = MFMA(k0[2 * hp + 1], qf[2 * hp + 1], c0), b1 = MFMA(k1[2 * hp + 1], qf[2 * hp + 1], c1);
        if (VL == 1) rdv(2 * hp, 2 * hp + 2);
        if (VL == 2 && hp == 1) rdv(0, 4);
        float t[4];
#pragma unroll
        for (int i = 0; i < 4; ++i) {
            auto r = __builtin_amdgcn_permlane32_swap(__float_as_uint(vmax(a0[i], a1[i])), __float_as_uint(vmax(b0[i], b1[i])), false, false);
            t[i] = vmax(__uint_as_float(r[0]), __uint_as_float(r[1]));
        }
#pragma unroll
        for (int i = 0; i < 2; ++i) {
            unsigned a = pk2(t[2 * i], t[2 * i + 1]);
            a = pk2(__uint_as_float(a << 16) * scale, __uint_as_float(a & 0xffff0000u) * scale);
            l[hp][2 * i] = __uint_as_float(a << 16);
            l[hp][2 * i + 1] = __uint_as_float(a & 0xffff0000u);
        }
        mx[hp] = vmax(vmax3(l[hp][0], l[hp][1], l[hp][2]), l[hp][3]);
    }
    {
        auto r = __builtin_amdgcn_permlane16_swap(__float_as_uint(mx[0]), __float_as_uint(mx[1]), false, false);
        const float c = vmax(__uint_as_float(r[0]), __uint_as_float(r[1])) * -log2e;
        auto q2 = __builtin_amdgcn_permlane16_swap(__float_as_uint(c), __float_as_uint(c), false, false);
        mx[0] = __uint_as_float(q2[0]); mx[1] = __uint_as_float(q2[1]);
    }
    float sm[2];
#pragma unroll
    for (int hp = 0; hp < 2; ++hp) {
#pragma unroll
        for (int i = 0; i < 4; ++i) l[hp][i] = __builtin_amdgcn_exp2f(__builtin_fmaf(l[hp][i], log2e, mx[hp]));
        sm[hp] = (l[hp][0] + l[hp][1]) + (l[hp][2] + l[hp][3]);
    }
    {
        auto r = __builtin_amdgcn_permlane16_swap(__float_as_uint(sm[0]), __float_as_uint(sm[1]), false, false);
        const float c = __builtin_amdgcn_rcpf(__uint_as_float(r[0]) + __uint_as_float(r[1]));
        auto q2 = __builtin_amdgcn_permlane16_swap(__float_as_uint(c), __float_as_uint(c), false, false);
        sm[0] = __uint_as_float(q2[0]); sm[1] = __uint_as_float(q2[1]);
    }
    FTA(2);
    const f32x4 z = {0.f, 0.f, 0.f, 0.f};
    const __amdgpu_buffer_rsrc_t rs_o = __builtin_amdgcn_make_buffer_rsrc(o, 0, 2048 * 512 * 2, 0x00020000);
#pragma unroll
    for (int hp = 0; hp < 2; ++hp) {
        const unsigned p01 = pk2(l[hp][0] * sm[hp], l[hp][1] * sm[hp]), p23 = pk2(l[hp][2] * sm[hp], l[hp][3] * sm[hp]);
        auto e01 = __builtin_amdgcn_permlane32_swap(p01, p01, false, false);
        auto e23 = __builtin_amdgcn_permlane32_swap(p23, p23, false, false);
#pragma unroll
        for (int e = 0; e < 2; ++e) {
            const int hh = 2 * hp + e;
            u32x4 pw = {e01[e] & m0[0], e23[e] & m0[1], e01[e] & m1[0], e23[e] & m1[1]};
            const bf16x8 pf = __builtin_bit_cast(bf16x8, pw);
            const f32x4 o0 = MFMA(v0[hh], pf, z), o1 = MFMA(v1[hh], pf, z);
            st16_sc1(rs_o, og + hh * 1024u, u32x4{pk2(o0[0], o0[1]), pk2(o0[2], o0[3]), pk2(o1[0], o1[1]), pk2(o1[2], o1[3])});
        }
    }
    FTA(4);
    if (w >= 6) lds_add(cnt + 7);
    {
        const Hs me = {hs.base, (unsigned)(HS_O + rt * 256 + (w >> 2) * 64 + hg * 16 + (w & 3) * 4), 0u};
        wait_vm<0>();
        hs_add(me, 1ull);
    }
    FTA(5);
    if (w >= 4) {
#pragma unroll 1
        for (int t = 12; t > 0; --t) glds16a(woc + t * (32 * 896), wv, smem + W_A + t * 4096 + w * 1024);
        lds_spin(cnt + 7, 2u);
        glds16a(woc - 32 * 896, wv, smem + W_A - 4096 + w * 1024);
        glds16a(woc, wv, smem + W_A + w * 1024);
        wait_vm<0>();
        lds_add(cnt + 4);
        END();
    }
    return hv3;
}
#define PA 10
#define NI 8
#define PD3 1
#define KRO 1
#define LR 2
template <class F>
__device__ __forceinline__ void oproj(char* smem, int wb, int so, u16* __restrict__ o, u16* __restrict__ out, int rt, int rg, int ct, int lane, F wait
    ) {
    const int fr = lane & 15, fq = lane >> 4;
    const int q = (lane >> 2) & 3, p = lane & 3, s = (2 * fq + (q >> 1)) & 3;
    const int rot = KRO ? (ct & 1) * 8 : 0;
    unsigned aw[2][4];
#pragma unroll
    for (int c = 0; c < 4; ++c) {
        aw[0][c] = wb + rot * 3584 + (4 * fq + q) * 128 + ((c ^ s) * 32) + 8 * p;
        aw[1][c] = aw[0][c] + (fq < 3 ? 2048 : 0);
    }
    bf16x8 fb[16][4];
    auto rdw = [&](int h) {
        if (KRO && h == 8) {
#pragma unroll
            for (int c = 0; c < 4; ++c) { aw[0][c] -= rot * 7168; aw[1][c] -= rot * 7168; }
        }
#pragma unroll
        for (int c = 0; c < 4; ++c) fb[h][c] = cat(lds_tr(smem + aw[0][c ^ (h & 1) * 2] + h * 3584), lds_tr(smem + aw[1][c ^ (h & 1) * 2] + h * 3584));
    };
    __amdgpu_buffer_rsrc_t rs_o = __builtin_amdgcn_make_buffer_rsrc(o, 0, 2048 * 512 * 2, 0x00020000);
    __amdgpu_buffer_rsrc_t rs_out = __builtin_amdgcn_make_buffer_rsrc(out, 0, 2048 * 896 * 2, 0x00020000);
    unsigned ga = (unsigned)(rt * 8 + rg) * 16384u + (unsigned)lane * 16u;
    unsigned sw = so + fr * O_P + fq * 8, sr = so + (lane >> 3) * O_P + (lane & 7) * 16;
    unsigned ob = (unsigned)((rt * 128 + (rg >> 1) * 32 + 2 * (lane >> 3) + (rg & 1)) * 1792 + ct * 128 + (lane & 7) * 16);
    f32x4 acc[4];
#pragma unroll
    for (int c = 0; c < 4; ++c) { acc[c] = f32x4{0.f, 0.f, 0.f, 0.f}; asm volatile("" : "+v"(acc[c]), "+v"(aw[0][c]), "+v"(aw[1][c])); }
    asm volatile("" : "+v"(ob), "+v"(sw), "+v"(sr), "+v"(ga));
    int so4[4];
#pragma unroll
    for (int i = 0; i < 4; ++i) so4[i] = (i << 12) ^ rot << 10;
    asm volatile("" : "+s"(rs_o), "+s"(rs_out), "+s"(so4[0]), "+s"(so4[1]), "+s"(so4[2]), "+s"(so4[3]));
    wait(0);
    bf16x8 fa[16];
    auto lda = [&](int ks) { fa[ks] = __builtin_bit_cast(bf16x8, __builtin_amdgcn_raw_buffer_load_b128(rs_o, ga + (ks & 3) * 1024u, so4[ks >> 2], 16)); };
#pragma unroll
    for (int ks = 0; ks < NI; ++ks) lda(ks);
    SB(); QT(4);
    wait(1);
#pragma unroll
    for (int ks = 0; ks < PA; ++ks) rdw(ks);
    SB(); QT(5);
#pragma unroll
    for (int ks = 0; ks < 16; ++ks) {
#pragma unroll
        for (int c = 0; c < 4; ++c) {
            acc[c] = MFMA(fb[ks][c], fa[ks], acc[c]);
            if (c < LR && ks * LR + c + NI < 16) { SB(); lda(ks * LR + c + NI); SB(); }
        }
        SB();
        if (ks + PA < 16) rdw(ks + PA);
        if (ks == 0) QT(6);
        if (ks == 4) QT(7);
        if (ks == 9) QT(8);
        if (ks == 15) QT(9);
        SB();
    }
    STAMP(1, 3);
#pragma unroll
    for (int c = 0; c < 4; ++c) *reinterpret_cast<u32x2*>(smem + sw + c * 32) = u32x2{pk2(acc[c][0], acc[c][1]), pk2(acc[c][2], acc[c][3])};
    asm volatile("" ::: "memory");
    const u32x4 r0 = *reinterpret_cast<const u32x4*>(smem + sr), r1 = *reinterpret_cast<const u32x4*>(smem + sr + 8 * O_P);
    SB();
    wait(2);
    __builtin_amdgcn_raw_buffer_store_b128(r0, rs_out, ob, 0, OPOL);
    __builtin_amdgcn_raw_buffer_store_b128(r1, rs_out, ob, 16 * 1792, OPOL);
}

__global__ __launch_bounds__(512) void kf(const u16* xA, const u16* wqkv, const char* xB, const char* Hr, const char* Hi, const char* Cr,
                                          const char* Ci, const char* wo, u16* out, char* ws) {
    __shared__ __attribute__((aligned(16))) char smem[LDS_TOTAL];
    int tid = threadIdx.x, bid = blockIdx.x;
    u64 pc0;
    {
        unsigned part, off;
        asm volatile("s_getpc_b64 %0\n\ts_bfe_u32 %1, %2, 0x30003\n\ts_mulk_i32 %1, %3" : "=&s"(pc0), "=&s"(part), "+s"(bid) : "i"(CODE_PART) : "scc");
        asm volatile("s_mov_b32 m0, %4\n\tv_mad_u32_u24 %0, %1, 12, %2\n\tglobal_load_lds_dword %0, %3" : "=&v"(off), "+v"(tid) : "s"(part), "s"(pc0), "i"(Q_DUMMY) : "memory");
        asm volatile("" : "+s"(bid));
    }
    __builtin_assume(tid >= 0 && tid < 512 && bid >= 0 && bid < 256);
    const int lane = tid & 63, xcd = bid & 7;
    int w = __builtin_amdgcn_readfirstlane(tid >> 6), j = bid >> 3;
    STAMP(0, 0);
    const int ks = xcd & 3, rl = j & 3, c6 = j >> 2, r8 = (xcd >> 2) * 4 + rl;
    BL q;
    if (j < 24 && w < 4) {
        unsigned ox = (unsigned)(((ks & 1) * 1024 + r8 * 64) * 896), oh = (unsigned)((c6 < 5 ? c6 * 80 : 384) * 896);
#define KR 1
        q.kc = c6 + KR * rl >= 7 ? c6 + KR * rl - 7 : c6 + KR * rl;
        b_off(q, c6 < 4 ? 79 : 63, lane, w);
        const char *p0 = xB, *p1 = Hr, *p2 = Hi;
        asm volatile("" : "+v"(q.offX), "+v"(q.offH[0]), "+v"(q.offH[1]), "+v"(q.offH[2]));
        asm volatile("" : "+s"(p0), "+s"(p1), "+s"(p2), "+s"(ox), "+s"(oh), "+s"(q.kc));
        q.xg = p0 + ox;
        q.hg = ((ks >> 1) ? p2 : p1) + oh;
        STAMP(0, 7);
        reqH(q, smem, w, 0); reqX(q, smem, w);
        reqH(q, smem, w, 1); reqX(q, smem, w);
        asm volatile("" ::"s"(xA), "s"(wqkv), "s"(Cr), "s"(Ci), "s"(wo), "s"(out), "s"(ws));
    }
    asm volatile("" : "+s"(w), "+s"(j));
    __builtin_assume(w >= 0 && w < 8 && j >= 0 && j < 32);
    if (w == 7) *reinterpret_cast<unsigned*>(smem + L_GO + (lane & 7) * 4) = 0u;
    Hs hs;
    hs.base = (const unsigned*)(ws + WS_HS); hs.off = 0; hs.snap = 0;
    int ro = 0, rg = 0, ct = 0;
    bool duty = true;
    u64 again = 0;
    unsigned ka = 0, kb = 0;
    int wb = W_0;
    unsigned full = 4u;
    if (j < 24) {
        u16* ab = (u16*)ws;
        u16* px = (u16*)(ws + WS_PX);
        const int rbl = j & 7, cg = j >> 3, rb = (xcd >> 2) * 8 + rbl;
#define DU 1
        const int un = DU ? j - min(c6, 4) : j - 4;
        ro = xcd * 2 + (un >> 1 & 1); ct = un >> 2; rg = (un & 1) * 4 + (w & 3);
        const int g1 = ks * 3 + cg;
        if (w < 4) {
            const char* cgp = ((ks >> 1) ? Ci : Cr) + (long)rb * (128 * 4096) + (ks & 1) * 2048;
            b_load(smem, q, cgp, (const char*)ab + g1 * (16 * B_SL), (u64)(((cg == 0 ? TB0 : cg == 1 ? TB1 : TB2) + rbl * 0x11111111u) & 0x77777777u) << 32 |
#define OWN 0
                   early(((cg == 0 ? TC0 : cg == 1 ? TC1 : TC2) + (RA >> 4 * rbl & 7) * 0x11111111u) & 0x77777777u), ks, cg == 2,
                   wo + ct * 128, (j & 19) != 3 ? M_OR : 0ull, hs, lane, w, r8 * 2 + (c6 & 1)
                   );
        } else {
            const int cw = w - 4;
            asm volatile("" ::"s"(xB), "s"(Hr), "s"(Hi));
            hs.off = hs_off((lane & 56) == 32 ? (lane & 60) | ((ks - lane) & 3) : lane, g1 * 256, HS_X + ((rb * 3 + cg) * 4 + cw) * 256, ro, ct * 2 + (un & 1));
            unsigned v1 = 0;
            hs_rmw(hs, v1, w == 5 ? M_LO | M_HI | M_XW | M_XR | M_OW : M_XW | M_XR);
            STAMP(0, 7);
            auto post = [&]() {
                if (w == 5) *reinterpret_cast<u32x2*>(smem + L_KEY + lane * 8) = u32x2{hs.off, hs_take<0>(v1)};
            };
            const unsigned dbase = (unsigned)((g1 * 16 + r8 * 2) * B_SL + (c6 & 1) * (c6 < 4 ? 80 : 64) * 64);
            const unsigned sg = (ks >> 1) ? 0x80008000u : 0u;
            if (c6 < 4 && !(cw & 1)) ab_mma<3>(smem, dbase, sg, lane, cw >> 1, 0, ab, post);
            else ab_mma<2>(smem, dbase, sg, lane, cw >> 1, (cw & 1) * (c6 < 4 ? 3 : 2), ab, post);
            hs.snap = hs_take<63>(v1);
            hs_add(hs, 1ull << 36);
            const unsigned pxb = (unsigned)(rb * 3 + cg) * 131072u, ob0 = (unsigned)((rb * 128 + ks * 16) * 896 + 448 + cg * 160) * 2u;
            if (cg == 2) y_mma<4>(smem, lane, cw, px, pxb, ks, out, ob0, hs);
            else y_mma<5>(smem, lane, cw, px, pxb, ks, out, ob0, hs);
            duty = false;
        }
    } else {
        const int qi = j - 24;
        if (AD) __builtin_amdgcn_s_sleep(AD);
#define AM 1
        const int hg = xcd & 3, rt = (xcd >> 2) * 8 + qi;
#define XT4 0x5401
        int kst = qi + (XT4 >> (4 * hg) & 15); kst = kst >= 7 ? kst - 7 : kst;
        ro = xcd * 2 + (qi >> 1 & 1); ct = 5 + (qi >> 2); rg = (qi & 1) * 4 + (w & 3); duty = w < 4;
        const unsigned hv = qkv_attn(smem, xA, wqkv, wo, ws, rt, hg, tid, w, hs, kst, ro, ct * 2 + (qi & 1),
                                     (u64)(((ks >> 1) ? Ci : Cr) + (long)rt * (128 * 4096) + (ks & 1) * 2048 + 1024), qi, wo + ct * 128);
        STAMP(0, 21);
        if (w == 0 && !hs_new(hs, hv, M_R14)) again = 0xf000full << 4 * hg;
        wb = W_A; full = 8u;
    }
    if (duty) {
        QDECL;
        QT(0);
        if (w == 0) hs_rmw(hs, ka, M_OW);
        if (j < 24) {
            const unsigned offW = wo_off(lane, w * 32);
#pragma unroll
            for (int n = WP2; n < 14; ++n) wo_req(smem, wo + ct * 128, offW, w, n);
        }
        SB();
        auto wait = [&](int ph) {
            volatile LDS_AS unsigned* go = (volatile LDS_AS unsigned*)(smem + L_GO);
            if (ph == 0) {
                if (w == 0) { __builtin_amdgcn_s_sleep(PD3); hs_wait2(hs, M_OW, ka, kb); lds_add(go); } else wait_vm<0>();
                lds_add(go + 4);
                lds_spin<GSL>(go, 9u - full);
                STAMP(1, 0); QT(1);
            } else if (ph == 1) {
                if (w == 0) hs_add(hs, M_OR);
                lds_spin(go + 4, full);
            } else if (GT && j < 24) lds_spin<GSL>(go + 5, GT);
        };
        oproj(smem, wb, O_ST + (w & 3) * (16 * O_P), (u16*)(ws + WS_O), out, ro, rg, ct, lane, wait
              );
        QT(2);
        asm volatile("s_cmp_eq_u64 %0, 0\n\ts_cbranch_scc0 1f\n\ts_endpgm\n1:" ::"s"(again), "v"(ka), "v"(kb) : "memory", "scc");
        hs_keep(ka, kb);
        QT(3); QOUT(10);
        if (again) { hs_wait(hs, M_R14, M_R14, 0u); hs_add(hs, again); }
    }
    STAMP(1, 24);
}

extern "C" void kernel_launch(void* const* d_in, const int* in_sizes, int n_in, void* d_out, int out_size,
                              void* d_ws, size_t ws_size, hipStream_t stream) {
    kf<<<256, 512, 0, stream>>>((const u16*)d_in[0], (const u16*)d_in[1], (const char*)d_in[3], (const char*)d_in[6], (const char*)d_in[7],
                                (const char*)d_in[4], (const char*)d_in[5], (const char*)d_in[2], (u16*)d_out, (char*)d_ws);
}
```

```cpp
#include <hip/hip_runtime.h>
#include <hip/hip_bf16.h>

typedef unsigned short u16;
typedef __attribute__((ext_vector_type(8))) short bf16x8;
typedef __attribute__((ext_vector_type(4))) short s16x4;
typedef __attribute__((ext_vector_type(4))) float f32x4;
typedef __attribute__((ext_vector_type(2))) __bf16 bf2;
typedef __attribute__((ext_vector_type(2))) unsigned u32x2;
typedef __attribute__((ext_vector_type(4))) unsigned u32x4;

#define LDS_AS __attribute__((address_space(3)))
#define GLB_AS __attribute__((address_space(1)))

constexpr int D = 448;

#define STAMP(kern, idx)
#define STAMPC(kern, idx)
#define QDECL
#define QT(i)
#define QOUT(base)

typedef __attribute__((ext_vector_type(2))) float f32x2;
__device__ __forceinline__ unsigned pk2(float a, float b) {
    f32x2 v = {a, b};
    return __builtin_bit_cast(unsigned, __builtin_convertvector(v, bf2));
}
__device__ __forceinline__ void glds16a(const void* sbase, unsigned voff, const char* l) {
    const unsigned la = (unsigned)(size_t)(LDS_AS const char*)l;
    asm volatile("s_mov_b32 m0, %2\n\ts_nop 0\n\tglobal_load_lds_dwordx4 %0, %1" ::"v"(voff), "s"(sbase), "s"(la) : "memory");
}
#define NTS " nt"
__device__ __forceinline__ void glds16n(const void* sbase, unsigned voff, const char* l) {
    const unsigned la = (unsigned)(size_t)(LDS_AS const char*)l;
    asm volatile("s_mov_b32 m0, %2\n\ts_nop 0\n\tglobal_load_lds_dwordx4 %0, %1" NTS ::"v"(voff), "s"(sbase), "s"(la) : "memory");
}
__device__ __forceinline__ float vmax(float a, float b) { return __builtin_elementwise_maximum(a, b); }
__device__ __forceinline__ float vmax3(float a, float b, float c) { return vmax(vmax(a, b), c); }
__device__ __forceinline__ bf16x8 lds_rd128(const char* p) { return *reinterpret_cast<const bf16x8*>(p); }
__device__ __forceinline__ s16x4 lds_tr(const char* p) {
    return __builtin_amdgcn_ds_read_tr16_b64_v4i16((LDS_AS s16x4*)p);
}
__device__ __forceinline__ bf16x8 cat(s16x4 a, s16x4 b) {
    bf16x8 r;
    r[0] = a[0]; r[1] = a[1]; r[2] = a[2]; r[3] = a[3];
    r[4] = b[0]; r[5] = b[1]; r[6] = b[2]; r[7] = b[3];
    return r;
}
#define MFMA(a, b, c) __builtin_amdgcn_mfma_f32_16x16x32_bf16((a), (b), (c), 0, 0, 0)
template <int N> __device__ __forceinline__ void wait_vm() { asm volatile("s_waitcnt vmcnt(%0)" ::"i"(N) : "memory"); }
#define SB() __builtin_amdgcn_sched_barrier(0)
#define EN 1
#define END() do { if (EN) asm volatile("s_endpgm" ::: "memory"); } while (0)
template <int NP, int NM> __device__ __forceinline__ void ilv() {
#pragma unroll
    for (int i = 0; i < NP; ++i) {
        __builtin_amdgcn_sched_group_barrier(0x8, 1, 0);
        __builtin_amdgcn_sched_group_barrier(0x100, 1, 0);
    }
    if constexpr (NM > 0) __builtin_amdgcn_sched_group_barrier(0x8, NM, 0);
}
#define BAR()                                               \
    do {                                                    \
        asm volatile("s_waitcnt lgkmcnt(0)" ::: "memory");  \
        __builtin_amdgcn_s_barrier();                       \
        asm volatile("" ::: "memory");                      \
    } while (0)
#define BARL()                                              \
    do {                                                    \
        asm volatile("" ::: "memory");                      \
        __builtin_amdgcn_s_barrier();                       \
        asm volatile("" ::: "memory");                      \
    } while (0)

__device__ __forceinline__ int kperm(int r) { return (r & ~12) | ((r & 4) << 1) | ((r & 8) >> 1); }

constexpr int LDS_TOTAL = 163840;
constexpr int WS_O = 4 << 20, WS_PX = 8 << 20, WS_HS = 16 << 20;
#define CODEMAX 40192u
constexpr unsigned CODE_MAX = CODEMAX;

typedef unsigned long long u64;
struct Hs {
    const unsigned* base;
    unsigned off, snap;
};
constexpr int HS_O = 16 * 256, HS_X = 32 * 256;
constexpr u64 M_LO = 0xffffull, M_HI = 0xffff0000ull, M_OW = 0xffffull << 48, M_OR = 1ull << 47, M_R14 = 0x3fffull << 32;
constexpr u64 M_XW = 0xfull << 32, M_XR = 0xf0ull << 32, M_XO = 0xeull << 32;
__device__ __forceinline__ unsigned hs_off(int lane, int b, int bx, int ro, int co) {
    const int bo = HS_O + ro * 256;
    return (unsigned)(lane * 4 + (lane < 32 ? b : lane < 47 ? bx - 32 * 4 : lane == 47 ? bo + 128 - 47 * 4 + co * 4 : bo - 48 * 4 + (co & 1) * 64));
}
template <int N> __device__ __forceinline__ unsigned hs_take(unsigned v) {
    unsigned r;
    asm volatile("s_waitcnt vmcnt(%2)\n\tv_mov_b32 %0, %1" : "=v"(r) : "v"(v), "i"(N) : "memory");
    return r;
}
__device__ __forceinline__ void hs_rmw(const Hs& h, unsigned& v, u64 m, unsigned d = 0u) {
    asm volatile("s_mov_b64 exec, %4\n\tglobal_atomic_add %0, %1, %2, %3 sc0 sc1\n\ts_mov_b64 exec, -1" : "+v"(v) : "v"(h.off), "v"(d), "s"(h.base), "s"(m) : "memory");
}
__device__ __forceinline__ void hs_add(const Hs& h, u64 m) {
    const unsigned one = 1u;
    asm volatile("s_mov_b64 exec, %3\n\tglobal_atomic_add %0, %1, %2 sc1\n\ts_mov_b64 exec, -1" ::"v"(h.off), "v"(one), "s"(h.base), "s"(m) : "memory");
}
__device__ __forceinline__ bool hs_new(const Hs& h, unsigned v, u64 m) { return (__ballot(v == h.snap) & m) == 0; }
#define PSL 2
__device__ __forceinline__ void hs_wait(const Hs& h, u64 m, u64 e, unsigned d) {
#pragma unroll 1
    for (unsigned spin = 0; spin < 40000u; ++spin) {
        unsigned v = 0;
        hs_rmw(h, v, e, d);
        if (hs_new(h, hs_take<0>(v), m)) return;
        __builtin_amdgcn_s_sleep(PSL);
    }
}
#define PD2 8
__device__ __forceinline__ void hs_wait2(const Hs& h, u64 m, unsigned& a, unsigned& b) {
    unsigned n = 40000u;
    unsigned z = 0u;
    asm volatile("s_mov_b64 exec, %7\n"
                 "1:\n\tglobal_atomic_add %1, %3, %4, %5 sc0 sc1\n\ts_waitcnt vmcnt(1)\n\tv_cmp_eq_u32 vcc, %0, %6\n\ts_cbranch_vccz 2f\n\t"
                 "global_atomic_add %0, %3, %4, %5 sc0 sc1\n\ts_waitcnt vmcnt(1)\n\tv_cmp_eq_u32 vcc, %1, %6\n\ts_cbranch_vccz 2f\n\t"
                 "s_sub_u32 %2, %2, 1\n\ts_cbranch_scc0 1b\n"
                 "2:\n\ts_mov_b64 exec, -1"
                 : "+&v"(a), "+&v"(b), "+&s"(n) : "v"(h.off), "v"(z), "s"(h.base), "v"(h.snap), "s"(m) : "memory", "vcc", "scc");
}
__device__ __forceinline__ void hs_wait3(const Hs& h, u64 m, unsigned& a, unsigned& b) {
    unsigned n = 40000u;
    unsigned z = 0u;
    asm volatile("s_mov_b64 exec, %7\n\ts_branch 3f\n"
                 "1:\n\tglobal_atomic_add %1, %3, %4, %5 sc0 sc1\n\ts_waitcnt vmcnt(1)\n\tv_cmp_eq_u32 vcc, %0, %6\n\ts_cbranch_vccz 2f\n\t"
                 "global_atomic_add %0, %3, %4, %5 sc0 sc1\n"
                 "3:\n\ts_waitcnt vmcnt(1)\n\tv_cmp_eq_u32 vcc, %1, %6\n\ts_cbranch_vccz 2f\n\t"
                 "s_sub_u32 %2, %2, 1\n\ts_cbranch_scc0 1b\n"
                 "2:\n\ts_mov_b64 exec, -1"
                 : "+&v"(a), "+&v"(b), "+&s"(n) : "v"(h.off), "v"(z), "s"(h.base), "v"(h.snap), "s"(m) : "memory", "vcc", "scc");
}
template <int SL = 0>
__device__ __forceinline__ void lds_spin(volatile LDS_AS unsigned* p, unsigned t) {
    unsigned n = 1000000u, v;
    if (SL == 0)
        asm volatile("1:\n\tds_read_b32 %0, %2\n\ts_sub_u32 %1, %1, 1\n\ts_cbranch_scc1 2f\n\ts_waitcnt lgkmcnt(0)\n\tv_cmp_gt_u32 vcc, %3, %0\n\ts_cbranch_vccnz 1b\n"
                     "2:\n\ts_waitcnt lgkmcnt(0)" : "=&v"(v), "+s"(n) : "v"(p), "s"(t) : "memory", "vcc", "scc");
    else
        asm volatile("s_branch 3f\n1:\n\ts_sleep %4\n3:\n\tds_read_b32 %0, %2\n\ts_sub_u32 %1, %1, 1\n\ts_cbranch_scc1 2f\n\ts_waitcnt lgkmcnt(0)\n\tv_cmp_gt_u32 vcc, %3, %0\n\ts_cbranch_vccnz 1b\n"
                     "2:\n\ts_waitcnt lgkmcnt(0)" : "=&v"(v), "+s"(n) : "v"(p), "s"(t), "i"(SL) : "memory", "vcc", "scc");
}
__device__ __forceinline__ void lds_add(volatile LDS_AS unsigned* p) {
    const unsigned one = 1u;
    asm volatile("s_mov_b64 exec, 1\n\tds_add_u32 %0, %1\n\ts_mov_b64 exec, -1" ::"v"(p), "v"(one) : "memory");
}
__device__ __forceinline__ void hs_keep(unsigned a, unsigned b) { asm volatile("s_waitcnt vmcnt(0)" ::"v"(a), "v"(b) : "memory"); }
__device__ __forceinline__ bf16x8 ld16_p(__amdgpu_buffer_rsrc_t r, unsigned off) {
    return __builtin_bit_cast(bf16x8, __builtin_amdgcn_raw_buffer_load_b128(r, off, 0, 16));
}
__device__ __forceinline__ void st16_out(__amdgpu_buffer_rsrc_t r, unsigned off, u32x4 v) {
#define OPOL 16
    __builtin_amdgcn_raw_buffer_store_b128(v, r, off, 0, OPOL);
}
__device__ __forceinline__ void st16_sc1(__amdgpu_buffer_rsrc_t r, unsigned off, u32x4 v) {
    __builtin_amdgcn_raw_buffer_store_b128(v, r, off, 0, 16);
}
__device__ __forceinline__ void glds16m(const void* sbase, unsigned voff, const char* l, u64 mask) {
    const unsigned la = (unsigned)(size_t)(LDS_AS const char*)l;
    asm volatile("s_mov_b32 m0, %2\n\ts_mov_b64 exec, %3\n\tglobal_load_lds_dwordx4 %0, %1\n\ts_mov_b64 exec, -1" ::"v"(voff), "s"(sbase), "s"(la), "s"(mask) : "memory");
}
__device__ __forceinline__ void glds16c(const void* sbase, unsigned voff, const char* l) {
    const unsigned la = (unsigned)(size_t)(LDS_AS const char*)l;
    asm volatile("s_mov_b32 m0, %2\n\ts_nop 0\n\tglobal_load_lds_dwordx4 %0, %1 sc1" ::"v"(voff), "s"(sbase), "s"(la) : "memory");
}
__device__ __forceinline__ void glds16mc(const void* sbase, unsigned voff, const char* l, u64 mask) {
    const unsigned la = (unsigned)(size_t)(LDS_AS const char*)l;
    asm volatile("s_mov_b32 m0, %2\n\ts_mov_b64 exec, %3\n\tglobal_load_lds_dwordx4 %0, %1 sc1\n\ts_mov_b64 exec, -1" ::"v"(voff), "s"(sbase), "s"(la), "s"(mask) : "memory");
}
__device__ __forceinline__ void touch_p(const void* p, char* smem, int where) {
    const unsigned la = (unsigned)(size_t)(LDS_AS const char*)(smem + where);
    asm volatile("s_mov_b32 m0, %1\n\ts_nop 0\n\tglobal_load_lds_dword %0, off" ::"v"(p), "s"(la) : "memory");
}

__device__ __forceinline__ void code_touch(u64 pc0, int part, int lane, char* l) {
    const unsigned la = (unsigned)(size_t)(LDS_AS const char*)l;
    asm volatile("s_mov_b32 m0, %2\n\ts_nop 0\n\tglobal_load_lds_dword %0, %1" ::"v"(min((unsigned)(part * 48 + min(lane, 47)) * 128u, CODE_MAX)), "s"(pc0 & ~127ull), "s"(la) : "memory");
}

constexpr int A_SL = 16384, B_R0 = 6 * A_SL, B_SL = 20480, X_SL = 8192, H_SL = 10240, H_0 = B_R0 - H_SL, MISC = B_R0 + 3 * B_SL;
#define F2 0
#define F3 1
#define KP 5
#define ET 1
constexpr int CODE_PART = (CODE_MAX - 6144) / 7 / 128 * 128;
#define DK 2
constexpr int Y_ST = 5 * A_SL + 2048, Y_P = 336, O_ST = 4 * A_SL + 8192, O_P = 144;
constexpr int T_DUMMY = MISC, L_KEY = MISC + 1024, L_GO = 163328, W_0 = A_SL;
__device__ __forceinline__ unsigned wo_off(int lane, int r) {
    return (unsigned)((r + (lane >> 3)) * 896 + ((lane & 7) ^ (((lane >> 4) & 3) << 1)) * 16);
}
#define SX 0, 4, 1, 1, 1, 0, 0, 0, 0, 0, 0
#define SC 0, 0, 0, 0, 0, 1, 1, 0, 0, 0, 0
#define W0X 1
#define NB15 2
#define PB2 14
#define DEF 1
#define BL16 1
#define WOL 0
#define RT1 0x420
#define RT2 0
#define DR 1
#define NTN 0
#define NTW 0
#define NTA ""
#define GVS 0
#define TCF " sc1"
#define TCH 0
#define RTC (TCH & 1 ? 8 + DK : 0)
#define TMK 0x94u
#define TEV 0x0b1807
#define WL 20
#define P3 9
#define T3O 4
#define T3D 0
#define TC0 0x76542310u
#define TC1 0x76530421u
#define TC2 0x43176502u
#define TB0 0x76543210u
#define TB1 0x76543210u
#define TB2 0x76543210u
#define PT 0x20110
#define TA0 0

#define TF 0
#define T0 0
#define T0B 0
#define TX 0
#define LP 2
#define DP 0
#define PRS 12
#define WP1 2
#define EL 14
#define HB 99
#define FB 3
#define YP 0
#define WM 0
#define GT 0
#define GS 4
#define GSL 2
#define AD 0
#define SNK 0
#define XE 1
#define XSL "2"
#define LT 1
#define CWT 1
#define BARC() do { if (CWT) BARL(); else BAR(); } while (0)
#define RPG 2
#define LW 0x1d
#define X2 0
#define X2S 4
#define EP 0
#define WP2 11
#define XP 3
#define TM -1
#define TW 6
#define VL 0
#define PRE 2
#define RCG 2
#define WE 0
#define XS 0
#define EPK 1
#define SP -1
#define PKB 6
constexpr int SCH_X[11] = {SX}, SCH_C[11] = {SC};
constexpr int sch_nc() { int n = 0; for (int i = 0; i < 11; ++i) n += SCH_C[i]; return n; }
constexpr int sch_ap() { int n = 0; for (int i = PB2 - 3; i < 11; ++i) n += 4 * SCH_C[i]; return n; }
constexpr int C2L = 3 - sch_nc();
static_assert(C2L == 0 || C2L == 1, "");
constexpr int sch_wait(int b, bool w0) {
    int n = b == 5 ? 5 : 0, m = 0;
    bool past = b < 7;
    for (int t = 4; t < b; ++t) {
        if (w0 && past) n += t == 8 + DK;
        for (int i = 0; i < SCH_X[t - 4]; ++i) { if (past) n += 2; if (m == b - 7) past = true; ++m; }
        if (past) n += 4 * SCH_C[t - 4];
        if (w0 && past && t == PB2) ++n;
        if (past && t == RTC) ++n;
    }
    return n;
}
template <int B> __device__ __forceinline__ void sch_w(int w) {
    if (sch_wait(B, true) != sch_wait(B, false) && w == 0) wait_vm<sch_wait(B, true)>();
    else wait_vm<sch_wait(B, false)>();
}
__device__ __forceinline__ void sch_wait_b(int b, int w) {
    switch (b) {
        case 5: sch_w<5>(w); break;
        case 6: sch_w<6>(w); break;
        case 7: sch_w<7>(w); break;
        case 8: sch_w<8>(w); break;
        case 9: sch_w<9>(w); break;
        case 10: sch_w<10>(w); break;
        case 11: sch_w<11>(w); break;
        case 12: sch_w<12>(w); break;
        case 13: sch_w<13>(w); break;
        default: break;
    }
}
#define RA 0x76543210u
#define PI 0
__device__ __forceinline__ unsigned early(unsigned t) { return PI ? (t & 0x11111111u) << 2 | (t >> 1 & 0x33333333u) : t; }
struct BL {
    const char *xg, *hg;
    unsigned offX, offH[3];
    int kc, sx;
};
__device__ __forceinline__ void reqX(BL& q, char* smem, int w) {
    const char* gx = q.xg + q.kc * 128;
    const char* la = smem + q.sx * X_SL + w * 2048;
    glds16a(gx, q.offX, la);
    glds16a(gx + 16 * 896, q.offX, la + 1024);
    q.kc = q.kc == 6 ? 0 : q.kc + 1; q.sx = q.sx == 5 ? 0 : q.sx + 1;
}
__device__ __forceinline__ void reqH(BL& q, char* smem, int w, int k) {
    const char* gh = q.hg + q.kc * 128;
    const char* lh = smem + H_0 + k * H_SL + w * 2560;
    glds16a(gh, q.offH[0], lh);
    glds16a(gh, q.offH[1], lh + 1024);
    glds16m(gh, q.offH[2], lh + 2048, 0xffffffffull);
}
__device__ __forceinline__ void b_off(BL& q, int hn, int lane, int w) {
    const int r8 = lane >> 3, c = (lane & 7) ^ r8;
    q.offX = (unsigned)(((w >> 1) * 32 + (w & 1) * 4 + r8 + (r8 & 4)) * 896 + c * 16);
#pragma unroll
    for (int i = 0; i < 3; ++i) q.offH[i] = (unsigned)(min(w * 20 + i * 8 + r8, hn) * 896 + (c ^ ((w & 1) * 4)) * 16);
    q.sx = 0;
}
__device__ __forceinline__ void wo_req(char* smem, const char* wog, unsigned offW, int w, int n) {
    if (n < 12) glds16a(wog + ((n >> 2) * 128 + (n & 3) * 8) * 896, offW, smem + W_0 + (n >> 2) * A_SL + w * 4096 + (n & 3) * 1024);
    else glds16a(wog + (384 - 16 * w + (n & 1) * 8) * 896, offW, smem + W_0 + 3 * A_SL + w * 2048 + (n & 1) * 1024);
}
__device__ __forceinline__ void b_load(char* smem, BL& q, const char* cg, const char* abg, u64 tb, int ks,
                                       bool narrow, const char* wog, u64 mduty, Hs& hs, int lane, int w, int pm
#define QL(i)
                                       ) {
    const int r8 = lane >> 3, c = (lane & 7) ^ r8;
    unsigned offA[4];
#pragma unroll
    for (int i = 0; i < 4; ++i)
        offA[i] = (unsigned)(((w >> 1) * 64 + (((w & 1) * 2 + (i >> 1) + ks) & 3) * 16 + (i & 1) * 8 + r8) * 4096 + c * 16);
    const int rb0 = w * (narrow ? 32 : 40);
    const unsigned offB = (unsigned)((c >> 2) * 10240 + (rb0 + r8) * 64 + (c & 3) * 16);
    const u64 mB = narrow ? 1ull : ~0ull;
    int sa = 3;
    u64 tc = tb | 0x8888888800000000ull;
    const unsigned t3 = (unsigned)tb;
    auto reqC = [&]() {
        const char* g = cg + ((unsigned)tc & 15) * 128;
        const char* la = smem + sa * A_SL + w * 4096;
#pragma unroll
        for (int i = 0; i < 4; ++i) glds16a(g, offA[i], la + i * 1024);
        sa = sa == 5 ? 0 : sa + 1; tc >>= 4;
    };
    int sb = 0;
    tb = tb << 1 | 0x1111111100000000ull;
    auto reqB = [&]() {
        const char* g = abg + ((unsigned)tb & 15) * B_SL;
        const char* lb = smem + B_R0 + sb * B_SL + rb0 * 128;
#pragma unroll
        for (int i = 0; i < 4; ++i) glds16c(g + i * 512, offB, lb + i * 1024);
        glds16mc(g + 2048, offB, lb + 4096, mB);
        sb = sb == 2 ? 0 : sb + 1; tb >>= 4;
    };
    auto step = [&]() { reqB(); reqC(); };
    unsigned vA[4], vB[4], lc;
    const char* gc;
    auto prepC = [&](int h) {
        if (h == 0) { gc = cg - 3072 + ((unsigned)tc & 15) * 128; tc >>= 4; asm volatile("" : "+s"(gc), "+s"(tc)); }
        else { lc = (unsigned)(size_t)(LDS_AS const char*)(smem + sa * A_SL + w * 4096); sa = sa == 5 ? 0 : sa + 1; asm volatile("" : "+s"(lc), "+s"(sa)); }
    };
    auto turn0 = [&]() {
#pragma unroll
        for (int i = 0; i < 4; ++i) { vA[i] = offA[i] + (3 - i) * 1024; vB[i] = offB + (3 - i) * 512; asm volatile("" : "+v"(vA[i]), "+v"(vB[i])); }
        prepC(0); prepC(1);
    };
    auto turn = [&]() {
        asm volatile("s_mov_b32 m0, %2\n\ts_nop 0\n\tglobal_load_lds_dwordx4 %0, %1" ::"v"(vA[0]), "s"(gc), "s"(lc) : "memory");
        SB();
        const char* g = abg - 1536 + ((unsigned)tb & 15) * B_SL;
        tb >>= 4;
        asm volatile("" : "+s"(g), "+s"(tb));
        asm volatile("global_load_lds_dwordx4 %0, %1 offset:1024" ::"v"(vA[1]), "s"(gc) : "memory");
        SB();
        unsigned lb = (unsigned)(size_t)(LDS_AS const char*)(smem + B_R0 + sb * B_SL + rb0 * 128);
        sb = sb == 2 ? 0 : sb + 1;
        asm volatile("" : "+s"(lb), "+s"(sb));
        asm volatile("global_load_lds_dwordx4 %0, %1 offset:2048" ::"v"(vA[2]), "s"(gc) : "memory");
        SB();
        unsigned l5 = lb + 2048;
        asm volatile("" : "+s"(l5));
        asm volatile("global_load_lds_dwordx4 %0, %1 offset:3072\n\ts_mov_b32 m0, %4\n\ts_waitcnt vmcnt(13)\n\ts_barrier\n\tglobal_load_lds_dwordx4 %2, %3 sc1"
                     ::"v"(vA[3]), "s"(gc), "v"(vB[0]), "s"(g), "s"(lb) : "memory");
        SB();
        prepC(0);
        asm volatile("global_load_lds_dwordx4 %0, %1 offset:1024 sc1" ::"v"(vB[1]), "s"(g) : "memory");
        SB();
        prepC(1);
        asm volatile("global_load_lds_dwordx4 %0, %2 offset:2048 sc1\n\tglobal_load_lds_dwordx4 %1, %2 offset:3072 sc1\n\t"
                     "s_mov_b32 m0, %4\n\ts_mov_b64 exec, %5\n\tglobal_load_lds_dwordx4 %3, %2 offset:2048 sc1\n\ts_mov_b64 exec, -1"
                     ::"v"(vB[2]), "v"(vB[3]), "s"(g), "v"(vB[0]), "s"(l5), "s"(mB) : "memory");
        SB();
    };
    auto stw = [&]() { wait_vm<13>(); };
    unsigned hv = 0;
    const unsigned own = (lane & 15) == pm;
    const unsigned offT = (unsigned)((pm & ~1) * B_SL + (w >> 1) * 10240 + ((pm & 1) * 2 + (w & 1)) * (narrow ? 2048 : 2560) + min(lane, narrow ? 15 : 19) * 128);
    auto pull = [&](int p) {
        asm volatile("s_mov_b32 m0, %2\n\ts_nop 0\n\tglobal_load_lds_dword %0, %1" TCF ::"v"(offT), "s"(abg + p * B_SL), "i"(T_DUMMY) : "memory");
    };
    STAMP(0, 8);
#pragma unroll 1
    for (int k = 2; k < 7; ++k) {
        wait_vm<5>();
        BARL();
        reqH(q, smem, w, k); reqX(q, smem, w);
    }
    QL(0);
    q.xg += 512 * 896;
#pragma unroll
    for (int i = 0; i < SCH_X[0]; ++i) reqX(q, smem, w);
#pragma unroll
    for (int i = 0; i < SCH_C[0]; ++i) reqC();
#pragma unroll
    for (int b = 5; b < 15; ++b) {
        sch_wait_b(b, w);
        BARL();
        if (b == 6) QL(1);
        if (b == 7) QL(2);
        if (b == 8 + DK) QL(3);
        if (b == 13) QL(4);
        if (b == 14) QL(5);
        if (b == 6) STAMP(0, 2);
        if (b == 7) STAMP(0, 14);
        if (b == 8 && w == 0) {
            const u32x2 t = *reinterpret_cast<const u32x2*>(smem + L_KEY + lane * 8);
            hs.off = t[0]; hs.snap = t[1];
        }
        if (b == 8 + DK) { STAMP(0, 3); if (w == 0) hs_add(hs, 1ull << pm); if (TCH & 1) pull(0); }
#pragma unroll
        for (int i = 0; i < SCH_X[b - 4]; ++i) reqX(q, smem, w);
#pragma unroll
        for (int i = 0; i < SCH_C[b - 4]; ++i) reqC();
        if (b == PB2 && w == 0) hs_rmw(hs, hv, M_LO);
    }
    {
        volatile LDS_AS unsigned* go = (volatile LDS_AS unsigned*)(smem + L_GO + 4);
        if (w == 0) {
            const u64 m3 = 3u << (t3 << 1 & 14) | 3u << (t3 >> 3 & 14) | 3u << (t3 >> 7 & 14);
            if (PB2 > 14 || !hs_new(hs, hs_take<sch_ap()>(hv), m3)) hs_wait(hs, m3, M_LO, own);
            *go = 1u;
        } else {
#pragma unroll 1
            for (unsigned spin = 0; spin < 400000u && *go == 0u; ++spin) if (!(GVS)) __builtin_amdgcn_s_sleep(1);
        }
        asm volatile("" ::: "memory");
    }
    STAMP(0, 4);
#define BARM() BARL()
#define FT(i)
    QL(6);
#define GV 0
#define G1 4
    reqB(); FT(1);
    if ((GV & 1) && w == 0) __builtin_amdgcn_s_sleep(G1);
    reqB(); FT(2);
    if ((GV & 8) && w == 0) __builtin_amdgcn_s_sleep(G1);
    if (!(GV & 2)) {
        if (w == 0) hs_rmw(hs, hv, M_LO);
        if (C2L) reqC();
    }
    QL(7);
    wait_vm<5 + ((GV & 2) ? 0 : 4 * C2L)>(); FT(4); BARM();
    QL(8);
    if (GV & 2) {
        if (w == 0) hs_rmw(hs, hv, M_LO);
        if (C2L) reqC();
    }
    if (DR == 0 && w == 0) hs_add(hs, 0x10001ull << pm | mduty);
    step();
    {
        const unsigned v = hs_take<9 + 4 * C2L>(hv);
        if (w == 0) { if (!hs_new(hs, v, M_LO)) hs_wait(hs, M_LO, DR ? M_LO : M_LO | M_HI, own); }
    }
    BARM();
    QL(9);
    if (w == 0) hs_add(hs, 0x10001ull << pm | mduty);
    reqB();
    turn0();
#pragma unroll 1
    for (int i = 2; i < 5; ++i) turn();
    if (w == 0) hs_rmw(hs, hv, M_LO | M_HI, own);
    turn();
    if (w == 0) { if (!hs_new(hs, hs_take<9>(hv), M_HI)) hs_wait(hs, M_HI, M_HI, 0u); }
#pragma unroll 1
    for (int i = 6; i < 14; ++i) turn();
    const unsigned offW = wo_off(lane, w * 32);
    asm volatile("s_cmp_eq_u64 %0, 0\n\ts_cbranch_scc0 1f\n\ts_waitcnt vmcnt(9)\n\ts_barrier\n\ts_waitcnt vmcnt(0)\n\ts_endpgm\n1:" ::"s"(mduty) : "memory", "scc");
#pragma unroll
    for (int n = 0; n < WP2; ++n) {
        if (n == WP1) { wait_vm<9 + WP1>(); BARM(); }
        wo_req(smem, wog, offW, w, n);
    }
    wait_vm<WP2>(); BARM();
}
template <int NE, class F>
__device__ __forceinline__ void ab_mma(char* smem, unsigned dbase, unsigned sg, int lane, int sh, int ef, u16* ab, F post) {
    const int fr = lane & 15, fq = lane >> 4;
    const int z0 = fr * 128 + ((fq ^ (fr & 7)) * 16), z1 = z0 ^ 64;
    unsigned a0 = sh * 4096 + z0, a1 = sh * 4096 + z1, b0 = H_0 + ef * 2048 + z0, b1 = H_0 + ef * 2048 + z1;
    unsigned doff = dbase + (unsigned)(sh * 10240 + (ef * 16 + fr) * 64 + fq * 16);
    asm volatile("" : "+v"(a0), "+v"(a1), "+v"(b0), "+v"(b1), "+v"(doff));
    const char *sa0 = smem + a0, *sa1 = smem + a1, *sb0 = smem + b0, *sb1 = smem + b1;
    const __amdgpu_buffer_rsrc_t rs = __builtin_amdgcn_make_buffer_rsrc(ab, 0, 192 * B_SL, 0x00020000);
    f32x4 acc[NE][2];
    const f32x4 zero = {0.f, 0.f, 0.f, 0.f};
    int hl = 7 * H_SL;
    asm volatile("" : "+s"(hl));
    bf16x8 xf[2][2][2], hf[2][2][NE];
    constexpr int NRD = 2 * (2 + NE), NMF = 4 * NE;
    int rx = 0;
    auto adv = [&]() {
        const int da = rx == 5 ? -5 * X_SL : X_SL;
        rx = rx == 5 ? 0 : rx + 1;
        sa0 += da; sa1 += da;
    };
    auto put = [&](int mi) {
        const f32x4 a = acc[mi][0], b = acc[mi][1];
        st16_sc1(rs, doff + mi * 1024u, u32x4{pk2(a[0], a[1]) ^ sg, pk2(a[2], a[3]) ^ sg, pk2(b[0], b[1]) ^ sg, pk2(b[2], b[3]) ^ sg});
    };
    auto chunk = [&](int kt) {
        const int set = kt & 1;
#pragma unroll
        for (int i = 0; i < (RPG == 1 ? NRD + 2 : NMF); ++i) {
            if (kt > 0 && i < NMF) {
                const int k2 = kt < 7 ? i / (2 * NE) : i >> 1 & 1, mi = kt < 7 ? i % (2 * NE) >> 1 : i >> 2, ni = i & 1;
                acc[mi][ni] = MFMA(xf[set ^ 1][k2][ni], hf[set ^ 1][k2][mi], (kt == 1 && k2 == 0 ? zero : acc[mi][ni]));
                SB();
                if (kt == 7 && (i & 3) == 3 && mi > 0) { put(mi - 1); SB(); }
            }
#pragma unroll
            for (int r = i * RPG; r < (i + 1) * RPG; ++r)
                if (kt < 7 && r < NRD) {
                    const int k2 = r / (2 + NE), f = r % (2 + NE);
                    if (f < 2) xf[set][k2][f] = lds_rd128((k2 ? sa1 : sa0) + f * 2048);
                    else hf[set][k2][f - 2] = lds_rd128((k2 ? sb1 : sb0) + (kt < 6 ? kt : -1) * H_SL + (kt < 6 ? 0 : hl) + (f - 2) * 2048);
                    SB();
                }
            if (kt < 7 && i == NRD / RPG) { adv(); SB(); }
        }
    };
#pragma unroll 1
    for (int p = 0; p < 2; ++p) {
#pragma unroll
        for (int kt = 0; kt < 8; ++kt) {
            if (kt == DK + 1 && p) wait_vm<0>();
            if (kt == KP + 1 && !p) post();
            if (!(LW >> kt & 1)) __builtin_amdgcn_s_waitcnt(0xc07f);
            if (kt < 7 || p) BARL();
            SB();
            chunk(kt);
        }
        put(NE - 1);
        SB();
        STAMP(0, 9 + p);
        doff += B_SL;
    }
}
__device__ __forceinline__ float lo16(unsigned v) { return __uint_as_float(v << 16); }
__device__ __forceinline__ float hi16(unsigned v) { return __uint_as_float(v & 0xffff0000u); }
#define RGR 0
#define GS2 1
#define RSP 2
template <int NF>
__device__ __forceinline__ void y_mma(char* smem, int lane, int cw, u16* px, unsigned pxb, int ks, u16* out, unsigned ob0, Hs& hs) {
    const int fr = lane & 15, fq = lane >> 4;
    const int mh = cw >> 1, nh = cw & 1;
    f32x4 acc[5][4];
#pragma unroll
    for (int a = 0; a < NF; ++a)
#pragma unroll
        for (int b = 0; b < 4; ++b) { acc[a][b] = f32x4{0.f, 0.f, 0.f, 0.f}; asm volatile("" : "+v"(acc[a][b])); }
    const int z0 = fr * 128 + ((fq ^ (fr & 7)) * 16), z1 = z0 ^ 64;
    unsigned a0 = 3 * A_SL + mh * 8192 + z0, a1 = 3 * A_SL + mh * 8192 + z1, b0 = B_R0 + nh * (NF * 2048) + z0, b1 = B_R0 + nh * (NF * 2048) + z1;
    unsigned pl = pxb + (unsigned)(cw * 8192 + lane * 16), ph = pl - (unsigned)(lane * 8);
    if (NF == 5) asm volatile("" : "+v"(ph));
    const unsigned one = fq == (fr >> 2) ? 0x3f80u << ((fr & 1) * 16) : 0u, d0 = (fr & 2) ? 0u : one, d1 = (fr & 2) ? one : 0u;
    u32x4 s0 = {d0, d1, 0u, 0u}, s1 = {0u, 0u, d0, d1};
    asm volatile("" : "+v"(a0), "+v"(a1), "+v"(b0), "+v"(b1), "+v"(pl), "+v"(s0), "+v"(s1));
    const char *sa0 = smem + a0, *sa1 = smem + a1, *sb0 = smem + b0, *sb1 = smem + b1;
    const __amdgpu_buffer_rsrc_t rs = __builtin_amdgcn_make_buffer_rsrc(px, 0, 48 * 4 * 32768, 0x00020000);
    const __amdgpu_buffer_rsrc_t rs_out = __builtin_amdgcn_make_buffer_rsrc(out, 0, 2048 * 896 * 2, 0x00020000);
    bf16x8 cf[2][4], xf[2][NF];
    constexpr int NRD = 4 + NF;
    auto rd1 = [&](int i, int set, int st) {
        if (i == 0) xf[set][0] = lds_rd128((set ? sb1 : sb0) + st * B_SL);
        else if (i < 5) cf[set][i - 1] = lds_rd128((set ? sa1 : sa0) + st * A_SL + (i - 1) * 2048);
        else xf[set][i - 4] = lds_rd128((set ? sb1 : sb0) + st * B_SL + (i - 4) * 2048);
    };
    int da = -3 * A_SL;
    auto half = [&](int set, int st, int tog, int lo = 0, int hi = 4, bool dr = false) {
        int r = 0, n = 0;
#pragma unroll
        for (int i = 0; i < 4 * NF; ++i) {
            if ((i & 3) < lo || (i & 3) >= hi) continue;
            const int x = i >> 2, y = SNK && hi - lo == 4 && (x & 1) ? 3 - (i & 3) : i & 3;
            acc[x][y] = MFMA(xf[set][x], cf[set][y], acc[x][y]);
            if (CWT) SB();
            if (r < NRD && n == r * RSP / 2) { rd1(r, set ^ 1, st); ++r; }
            if (CWT && i == 4 * NF - 2 && hi - lo == 4) __builtin_amdgcn_s_waitcnt(0xc07f);
            if (i == 4 * NF - (CWT == 1 ? 3 : 1) && dr) { SB(); wait_vm<0>(); }
            if (i == 4 * NF - 3 && tog == 1) sa0 += da;
            if (i == 4 * NF - 3 && tog == 2) { sa1 += da; da = -da; }
            ++n;
            SB();
        }
    };
    STAMP(0, 4);
    if (DR == 0) wait_vm<0>();
    if (YP == 2) __builtin_amdgcn_s_setprio(3);
    BARL();
    QDECL; QT(0);
    STAMP(0, 5); STAMPC(0, 3);
#pragma unroll
    for (int i = 0; i < NRD; ++i) rd1(i, 0, 0);
    SB();
#pragma unroll 1
    for (int t = 0; t < (DEF == 2 ? 4 : 5); ++t) {
        half(0, 0, 0, 0, 4, true);
        BARC();
        half(1, 1, 0); half(0, 1, 0); BARC();
        half(1, 2, 0); half(0, 2, 1); BARC();
        half(1, 0, 2);
    }
    bf16x8 kf[4];
    unsigned ka, kb = 0;
    asm volatile("v_mov_b32 %0, %1" : "=v"(ka) : "v"(hs.snap));
    hs_rmw(hs, kb, M_XR);
    half(0, 0, 0, 1, 3);
    STAMP(0, 18); STAMPC(0, 4); QT(1);
    if (YP == 1) __builtin_amdgcn_s_setprio(3);
    const int pw = ks * 32768;
    auto put = [&](int p, int h) {
        const f32x4 a = acc[h < 2 ? 2 * h : NF - 1][p], b = acc[h < 2 ? 2 * h + 1 : 0][p];
        if (h < 2) __builtin_amdgcn_raw_buffer_store_b128(u32x4{pk2(a[0], a[1]), pk2(a[2], a[3]), pk2(b[0], b[1]), pk2(b[2], b[3])}, rs, pl + h * 1024u, pw + (p - 1) * 2560, 16);
        else __builtin_amdgcn_raw_buffer_store_b64(u32x2{pk2(a[0], a[1]), pk2(a[2], a[3])}, rs, ph + 2048u, pw + (p - 1) * 2560, 16);
    };
#pragma unroll
    for (int s = 0; s < 6; ++s) {
        const int set = s == 2 || s == 4 ? 0 : 1, l = s < 2 ? s + 1 : s < 4 ? 3 : 0, p = s == 1 ? 1 : s == 2 ? 2 : s == 4 ? 3 : 0;
#pragma unroll
        for (int mi = 0; mi < NF; ++mi) {
            acc[mi][l] = MFMA(xf[set][mi], cf[set][l], acc[mi][l]);
            SB();
            if (p > 0 && mi >= 1 && mi < 3) put(p, mi - 1);
            if (NF == 5 && p >= 1 && mi == 3) put(p, 2);
            if (DEF == 2 && s == 5) xf[0][mi] = lds_rd128(sb0 + B_SL + mi * 2048);
            SB();
        }
        if (s == 4 && XE) { hs_rmw(hs, ka, M_XO); SB(); }
        if (s == GS) { lds_add((volatile LDS_AS unsigned*)(smem + L_GO)); SB(); }
    }
#pragma unroll
    for (int mi = 0; mi < NF; ++mi) asm volatile("" : "+v"(acc[mi][0]));
    STAMP(0, 19); QT(2);
    int pr[3];
#pragma unroll
    for (int l = 1; l < 4; ++l) pr[l - 1] = ((ks - l) & 3) * 32768 + (l - 1) * 2560;
    u32x2 sd = {s0[0], s0[1]};
    __amdgpu_buffer_rsrc_t ro2 = rs_out;
    asm volatile("" : "+s"(pr[0]), "+s"(pr[1]), "+s"(pr[2]), "+s"(ro2));
    asm volatile("" : "+v"(sd));
    unsigned l2 = __builtin_amdgcn_mbcnt_hi(~0u, __builtin_amdgcn_mbcnt_lo(~0u, 0u));
    unsigned yw = Y_ST + mh * (16 * Y_P) + nh * (NF * 32) + (l2 & 15) * Y_P + (l2 >> 4) * 8;
    unsigned yr = Y_ST + mh * (16 * Y_P) + nh * (NF == 5 ? 192 : 128) + (l2 >> 3) * Y_P + (l2 & 7) * 16;
    unsigned ys = Y_ST + mh * (16 * Y_P) + 128 + (l2 >> 2) * Y_P + (l2 & 3) * 16;
    unsigned ob = ob0 + mh * (64 * 1792) + nh * (NF == 5 ? 192 : 128) + (l2 >> 3) * 1792 + (l2 & 7) * 16;
    unsigned os = ob0 + mh * (64 * 1792) + 128 + (l2 >> 2) * 1792 + (l2 & 3) * 16;
    asm volatile("" : "+v"(yw), "+v"(yr), "+v"(ob));
    unsigned yc = (unsigned)(size_t)(LDS_AS char*)(smem + L_GO + 8 + mh * 4);
    if (NF == 5) asm volatile("" : "+v"(ys), "+v"(os), "+v"(yc));
    const bool all = hs_new(hs, hs_take<XE>(kb), M_XR);
    STAMP(0, 20); QT(3);
    hs_add(hs, 1ull << 32);
    u32x4 pv[3][2];
    u32x2 p5[3];
    unsigned pend = 14u, n = 200000u, t;
    const unsigned z = 0u;
#define X_LD(l, a, b, f, p) "s_bitcmp0_b32 %16, " #l "\n\ts_cbranch_scc1 1" #l "f\n\tbuffer_load_dwordx4 %" #a ", %21, %27, %" #p " offen sc1\n\t" \
    "buffer_load_dwordx4 %" #b ", %21, %27, %" #p " offen offset:1024 sc1\n\t" X_L5(f, p) "1" #l ":\n\t"
#define X_AD(c, l, a, b, f) c "s_waitcnt vmcnt(0)\n2" #l ":\n\t" \
    "v_mfma_f32_16x16x32_bf16 %0, %23, %" #a ", %0\n\tv_mfma_f32_16x16x32_bf16 %1, %24, %" #a ", %1\n\t" \
    "v_mfma_f32_16x16x32_bf16 %2, %23, %" #b ", %2\n\tv_mfma_f32_16x16x32_bf16 %3, %24, %" #b ", %3\n\t" X_A5(f)
#define X_ALL(W1, W2) asm volatile( \
    "1:\n\ts_mov_b32 exec_lo, 0\n\ts_mov_b32 exec_hi, 14\n" \
    "2:\n\tv_cmp_ne_u32 vcc, %14, %20\n\ts_and_b32 %16, vcc_hi, %15\n\ts_cbranch_scc1 3f\n\t" \
    "global_atomic_add %14, %18, %19, %26 sc0 sc1\n\ts_waitcnt vmcnt(20)\n\ts_sleep " XSL "\n\ts_sub_u32 %17, %17, 1\n\ts_cbranch_scc0 2b\n\ts_mov_b32 %16, %15\n" \
    "3:\n\ts_mov_b64 exec, -1\n\t" X_LD(1, 5, 6, 11, 28) X_LD(2, 7, 8, 12, 29) X_LD(3, 9, 10, 13, 30) \
    "s_xor_b32 %15, %15, %16\n\ts_cbranch_scc1 1b\n\t" \
    "s_bcnt1_i32_b32 %15, %16\n\ts_cmp_eq_u32 %15, 1\n\ts_waitcnt vmcnt(" W2 ")\n\ts_cbranch_scc0 4f\n\ts_waitcnt vmcnt(" W1 ")\n4:\n\t" \
    X_AD("s_bitcmp0_b32 %16, 1\n\ts_cbranch_scc1 21f\n\t", 1, 5, 6, 11) X_AD("s_cmp_eq_u32 %16, 8\n\ts_cbranch_scc1 22f\n\t", 2, 7, 8, 12) X_AD("", 3, 9, 10, 13) "s_nop " XN \
    : "+v"(acc[0][0]), "+v"(acc[1][0]), "+v"(acc[2][0]), "+v"(acc[3][0]), "+v"(acc[NF - 1][NF == 5 ? 0 : 1]), "=&v"(pv[0][0]), "=&v"(pv[0][1]), "=&v"(pv[1][0]), "=&v"(pv[1][1]), \
      "=&v"(pv[2][0]), "=&v"(pv[2][1]), "=&v"(p5[0]), "=&v"(p5[1]), "=&v"(p5[2]), "+&v"(ka), "+&s"(pend), "=&s"(t), "+&s"(n) \
    : "v"(hs.off), "v"(z), "v"(hs.snap), "v"(pl), "v"(ph), "v"(s0), "v"(s1), "v"(sd), "s"(hs.base), "s"(rs), "s"(pr[0]), "s"(pr[1]), "s"(pr[2]) : "memory", "vcc", "scc")
    if (NF == 5) {
#define X_L5(f, p) "buffer_load_dwordx2 %" #f ", %22, %27, %" #p " offen offset:2048 sc1\n"
#define X_A5(f) "v_mfma_f32_16x16x16_bf16 %4, %25, %" #f ", %4\n\t"
#define XN "3"
        X_ALL("3", "6");
#undef X_L5
#undef X_A5
    } else {
#define X_L5(f, p) "\n"
#define X_A5(f) ""
#undef XN
#define XN "4"
        X_ALL("2", "4");
#undef X_L5
#undef X_A5
    }
    u32x2 res[NF];
#pragma unroll
    for (int mi = 0; mi <= NF; ++mi) {
        if (mi < NF) { res[mi] = u32x2{pk2(acc[mi][0][0], acc[mi][0][1]), pk2(acc[mi][0][2], acc[mi][0][3])}; asm volatile("" : "+v"(res[mi])); }
        SB();
        if (mi > 0) *reinterpret_cast<u32x2*>(smem + yw + (mi - 1) * 32) = res[mi - 1];
        SB();
    }
    STAMP(1, 1); QT(5);
    if (NF == 5) {
        asm volatile("s_mov_b64 exec, 1\n\tds_add_rtn_u32 %0, %0, %1\n\ts_mov_b64 exec, -1" : "+v"(yc) : "v"(yc) : "memory");
    }
    asm volatile("" ::: "memory");
    const u32x4 r0 = *reinterpret_cast<const u32x4*>(smem + yr), r1 = *reinterpret_cast<const u32x4*>(smem + yr + 8 * Y_P);
    u32x4 r2;
    if (NF == 5) r2 = *reinterpret_cast<const u32x4*>(smem + ys);
    SB();
    __builtin_amdgcn_raw_buffer_store_b128(r0, ro2, ob, 0, OPOL);
    __builtin_amdgcn_raw_buffer_store_b128(r1, ro2, ob, 8 * 1792, OPOL);
    if (NF == 5) {
        unsigned second;
        asm volatile("v_readfirstlane_b32 %0, %1" : "=s"(second) : "v"(yc), "v"(r1[0]));
        if (second) __builtin_amdgcn_raw_buffer_store_b128(r2, ro2, os, 0, OPOL);
    }
    if (!all) hs_add(hs, 1ull << 32);
    END();
}

constexpr int Q_WSL = 21504, Q_AR = 5 * Q_WSL, Q_ASL = 18432, QP = 784, Q_DUMMY = 162816, W_A = Q_DUMMY - 14 * 4096;
__device__ __forceinline__ void qkv_reqW(char* smem, const u16* __restrict__ wqkv, const unsigned (&offW)[6], int c, int kw, int sw) {
    const char* g = (const char*)wqkv + (long)kw * (32 * 1344 * 2);
    const char* sb = smem + sw * Q_WSL + c * 1024;
#pragma unroll
    for (int k = 0; k < 5; ++k) glds16a(g, offW[k], sb + k * 4096);
    if (c == 0) glds16a(g, offW[5], sb + 5 * 4096);
}
__device__ __forceinline__ void qkv_reqX(char* smem, const u16* __restrict__ xA, const unsigned (&offX)[5], int c, int kx, int sx) {
    const char* g = (const char*)xA + kx * 128;
    const char* sb = smem + Q_AR + sx * Q_ASL + c * 1024;
#pragma unroll
    for (int k = 0; k < 4; ++k) glds16a(g, offX[k], sb + k * 4096);
    if (c < 2) glds16a(g, offX[4], sb + 4 * 4096);
}
template <int NR, int NM, int HS, bool XT, class F, class G, class H>
__device__ __forceinline__ unsigned qkv_wave(char* smem, const u16* __restrict__ xA, const u16* __restrict__ wqkv,
                                         int w, int lane, int G0, int R0, Hs& hs, int k0,
                                         const unsigned (&offW)[6], const unsigned (&offX)[5], unsigned& apad, F prep, G setup, H wreq) {
    const int fr = lane & 15, fq = lane >> 4, c = w & 3;
    const int u = fq & 1, ah = fq >> 1, q = (lane >> 2) & 3, p = lane & 3;
    constexpr int N0 = HS < NM ? 0 : 1;
    constexpr int NX = NR + (XT ? 1 : 0), NW = NM + (XT ? 1 : 0);
    constexpr int NRD = NX - N0 + 2 * NW, NMF = NR * NM - HS + (XT ? 1 : 0);
    constexpr bool LATE = NR == 4, W0 = HS == 6;
    auto st = [&](int i) { const int k = k0 + i; return k >= 7 ? k - 7 : k; };
    f32x4 acc[NM * NR + 1];
#pragma unroll
    for (int a = 0; a < NM * NR + 1; ++a) acc[a] = f32x4{0.f, 0.f, 0.f, 0.f};
    const char* pw = smem + (16 * ah + 4 * u + q) * 672 + 8 * p + G0 * 32;
    const char* px0 = smem + Q_AR + (R0 * 16 + fr) * 128 + ((fq ^ (fr & 7)) * 16);
    const char* px1 = smem + Q_AR + (R0 * 16 + fr) * 128 + (((4 + fq) ^ (fr & 7)) * 16);
    const int dwx = (20 - G0) * 32, dxx = c * 2048;
    bf16x8 xf[2][NX];
    s16x4 wl[2][NW], wh[2][NW];
    int cw = 0, cx = 0;
    auto rd1 = [&](int i, int odd) {
        const char* xa = odd ? px1 : px0;
        if (i < NX - N0) { const int ni = i + N0; xf[odd][ni] = lds_rd128(xa + (ni < NR ? ni * 2048 : dxx)); }
        else {
            const int k = i - (NX - N0), mi = k >> 1;
            const char* pp = pw + (mi < NM ? mi * 32 : dwx);
            if (k & 1) wh[odd][mi] = lds_tr(pp + 8 * 672); else wl[odd][mi] = lds_tr(pp);
        }
    };
    auto tick = [&](int set) {
        int i = 0;
#pragma unroll
        for (int a = 0; a < NM; ++a)
#pragma unroll
            for (int b = 0; b < NR; ++b)
                if (b > 0 || a >= HS) {
                    acc[a * NR + b] = MFMA(cat(wl[set][a], wh[set][a]), xf[set][b], acc[a * NR + b]);
                    if (i < NRD) rd1(i, set ^ 1);
                    ++i;
                    SB();
                }
        if (XT) acc[NM * NR] = MFMA(cat(wl[set][NW - 1], wh[set][NW - 1]), xf[set][NX - 1], acc[NM * NR]);
        SB();
    };
    auto advW = [&]() { pw += cw == 4 ? -4 * Q_WSL : Q_WSL; cw = cw == 4 ? 0 : cw + 1; };
    auto advX = [&]() { const int d = cx == 2 ? -2 * Q_ASL : Q_ASL; px0 += d; px1 += d; cx = cx == 2 ? 0 : cx + 1; };
    unsigned pco[NW];
    auto prep2 = [&]() {
        int l2 = lane;
        asm volatile("" : "+v"(l2));
        __builtin_assume(l2 >= 0 && l2 < 64);
        prep(l2);
        const unsigned f2 = l2 & 15, c4 = (l2 >> 4) * 4;
#pragma unroll
        for (int mi = 0; mi < NM; ++mi) {
            const unsigned col = (G0 + mi) * 16 + c4;
            pco[mi] = (R0 * 16 + f2) * QP + col * 2 + (col * 147u >> 12) * 8;
        }
        if (XT) pco[NW - 1] = ((5 + c) * 16 + f2) * QP + 11 * 64 + 24 + c4 * 2;
#pragma unroll
        for (int mi = 0; mi < NW; ++mi) asm volatile("" : "+v"(pco[mi]));
    };
#pragma unroll
    for (int a = 0; a < NM * NR + 1; ++a)
        if (a < NM * NR ? (a % NR > 0 || a / NR >= HS) : XT) asm volatile("" : "+v"(acc[a]));
    auto tch = [&](int pos) {
#pragma unroll
        for (int i = 0; i < (PT >> 4 * pos & 15); ++i) wreq(-2);
    };
    unsigned hv = 0;
    if (W0) { setup(); hs_rmw(hs, hv, M_R14 | M_OW); }
    if (SP < 0) prep2();
    if (LATE) { if (T0 == 1) wreq(-7); tch(0); __builtin_amdgcn_s_setprio(LP); wait_vm<(PT & 15) + (T0 == 1)>(); }
    else if (W0) wait_vm<7>();
    else wait_vm<5>();
    BAR();
    STAMP(0, 1); STAMPC(0, 3);
#pragma unroll
    for (int i = 0; i < NRD; ++i) rd1(i, 0);
    advW();
    if (LATE) { qkv_reqX(smem, xA, offX, c, st(1), 1); qkv_reqW(smem, wqkv, offW, c, 2 * st(1), 2); tch(3); if (T0 == 2) wreq(-7); }
    else { qkv_reqW(smem, wqkv, offW, c, 2 * st(1) + 1, 3); wait_vm<W0 ? 6 : 5>(); }
    BARL();
    STAMP(0, 9);
    SB();
    int sa = 4, sb = 0, sx = 2;
#pragma unroll 1
    for (int s = 0; s < 6; ++s) {
        if (XP && LATE && s < 5) {
            if (XP == 3) qkv_reqW(smem, wqkv, offW, c, 2 * st(s + 2), sa);
            if (XP != 5) qkv_reqX(smem, xA, offX, c, st(s + 2), sx);
            if (XP >= 4) qkv_reqW(smem, wqkv, offW, c, 2 * st(s + 2), sa);
            if (TX >> s & 1) wreq(TF ? -6 : -2);
            SB();
        }
        if (DP && LATE) { __builtin_amdgcn_s_setprio(0); SB(); }
        tick(0);
        if (DP && LATE) { __builtin_amdgcn_s_setprio(LP); SB(); }
        advW(); advX();
        if (LATE && s < 5) { if (XP < 3) qkv_reqW(smem, wqkv, offW, c, 2 * st(s + 2), sa); if (!XP || XP == 5) qkv_reqX(smem, xA, offX, c, st(s + 2), sx); if (TW == 5) wreq(-5); }
        else if (LATE) { wait_vm<(PT >> 4 & 15)>(); tch(2); }
        else wait_vm<0>();
        if (LATE) {
            int s2 = s;
            asm volatile("" : "+s"(s2));
            if (SP >= 0 && s2 == SP) prep2();
            if (TX >> s2 & 1) wait_vm<10>();
            else wait_vm<TW == 5 ? 10 : 9>();
        }
        BAR();
        if (!LATE && s < 5) qkv_reqW(smem, wqkv, offW, c, 2 * st(s + 2) + 1, sb);
        if (!LATE) {
            int s2 = s;
            asm volatile("" : "+s"(s2));
            if (SP >= 0 && s2 == SP) prep2();
            if (W0 && s2 == 4) { hs.snap = hs_take<63>(hv); hs_add(hs, M_OR); }
            if (TW == 0 && s2 == 5) wreq(-1);
        }
        SB();
        tick(1);
        advW();
        if (LATE && (PT >> 4 & 15)) { int s3 = s; asm volatile("" : "+s"(s3)); if (s3 == 4) tch(1); }
        sa = sa >= 3 ? sa - 3 : sa + 2; sb = sb >= 3 ? sb - 3 : sb + 2; sx = sx == 2 ? 0 : sx + 1;
    }
    STAMP(0, 2); STAMPC(0, 5);
    if (W0) { hv = 0; hs_rmw(hs, hv, M_R14); }
    if (TW == 1 || (TW == 0 && LATE)) wreq(-1);
#pragma unroll
    for (int i = 0; i < NRD; ++i) rd1(i, 1);
    BAR();
    STAMP(0, 7); wreq(-16);
    if (LATE) tch(4);
    if (TW == 2) wreq(-1);
    if (LATE) tch(5);
    if (!LATE) __builtin_amdgcn_s_setprio(3);
    char* pc[NW];
#pragma unroll
    for (int mi = 0; mi < NW; ++mi) pc[mi] = smem + pco[mi];
    char* pad = smem + apad;
    auto pos = [&](int g, int& mi, int& ni) {
        int n = 0;
        mi = NM; ni = NR;
#pragma unroll
        for (int a = 0; a < NM; ++a)
#pragma unroll
            for (int b = 0; b < NR; ++b)
                if (b > 0 || a >= HS) { if (n == g) { mi = a; ni = b; } ++n; }
    };
#pragma unroll
    for (int f = 0; f < NMF + FB; ++f) {
        int mi, ni, mj, nj;
        pos(f, mi, ni); pos(f - FB, mj, nj);
        f32x4& d = acc[mi < NM ? mi * NR + ni : NM * NR];
        const f32x4 e = acc[mj < NM ? mj * NR + nj : NM * NR];
        u32x2 t;
        if (f < NMF) d = MFMA(cat(wl[0][mi], wh[0][mi]), xf[0][ni], d);
        SB();
        if (f >= FB) { t = u32x2{pk2(e[0], e[1]), pk2(e[2], e[3])}; asm volatile("" : "+v"(t)); }
        SB();
        if (f < NMF) d = MFMA(cat(wl[1][mi], wh[1][mi]), xf[1][ni], d);
        SB();
        if (f >= FB) *reinterpret_cast<u32x2*>(pc[mj] + (mj < NM ? nj * 16 * QP : 0)) = t;
        else if (f < 2 || w < 2) *reinterpret_cast<u32x2*>(pad + f * 32 * QP) = u32x2{0u, 0u};
        if (LATE && f == HB) BARL();
        SB();
    }
    wreq(-17);
    return W0 ? hs_take<0>(hv) : 0u;
}

__device__ __forceinline__ unsigned qkv_attn(char* smem, const u16* xA, const u16* wqkv, const char* wo, char* ws, int rt, int hg, int tid, int w,
                                         Hs& hs, int kst, int ro, int co, u64 cb, int qi, const char* woc) {
    const int lane = tid & 63, s0 = rt * 128, c = w & 3;
    unsigned offW[6], offX[5];
    if (w >= 4) {
#pragma unroll
        for (int k = 0; k < 5; ++k) {
            const int ix = min((c + 4 * k) * 64 + lane, 1151), row = ix >> 3, ps = ix & 7;
            offX[k] = (unsigned)(max(s0 - 16 + row, 0) * D + (ps ^ (row & 7)) * 8) * 2u;
            if (k < 4 || c < 2) glds16a((const char*)xA + kst * 128, offX[k], smem + Q_AR + c * 1024 + k * 4096);
        }
    }
#pragma unroll
    for (int k = 0; k < 6; ++k) {
        const int id = min((c + 4 * k) * 64 + lane, 1343), r = id / 42, pos = id - r * 42, seg = pos / 14, cc = pos - seg * 14;
        offW[k] = (unsigned)(kperm(r) * 1344 + seg * 448 + hg * 112 + cc * 8) * 2u;
        if (k == 0) { asm volatile("" : "+v"(offW[0])); asm volatile("" : "+s"(wqkv)); }
        if (w < 4 && (k < 5 || c == 0)) glds16a((const char*)wqkv + (long)(2 * kst) * (32 * 1344 * 2), offW[k], smem + c * 1024 + k * 4096);
    }
    u16* o = (u16*)(ws + WS_O);
    u64 tc;
    unsigned tk, tf = TF;
    int wd = 0;
    if (w < 4) qkv_reqW(smem, wqkv, offW, c, 2 * kst + 1, 1);
    else {
        tc = cb + (unsigned)((c * 32 + (lane >> 1)) * 4096);
        tk = (unsigned)(qi + TA0 + (lane & 1)) * 128u;
        asm volatile("" : "+v"(tc), "+v"(tk));
        if (TW == 5) { wd = (qi * 392 + 392 - 20 * WL + c * WL + min(lane, WL - 1)) * 128; asm volatile("" : "+v"(wd)); }
    }
    auto setup = [&]() {
        int l2 = lane;
        asm volatile("" : "+v"(l2));
        __builtin_assume(l2 >= 0 && l2 < 64);
        hs.off = hs_off(l2, HS_O + rt * 256, HS_O + rt * 256 + 128, ro, co);
    };
    const int fq = lane >> 4;
    const int par = w & 1, qb = w >> 1;
    unsigned aq, ak[2], av[2], az[2], m0[2], m1[2], og, apad, wt, wv;
    f32x4 c0, c1;
    auto prep = [&](unsigned ln) {
        const unsigned fr = ln & 15, f4 = ln >> 4, p = ln & 3, lim = w < 4 ? 39u : 71u;
        aq = (16 + 2 * (16 * qb + fr) + par) * QP + f4 * 16;
        ak[0] = aq - 16 * QP + 256;
        ak[1] = (2 * min(16 * qb + 16 + fr, lim) + par) * QP + 256 + f4 * 16;
        av[0] = (2 * (16 * qb + (ln >> 2)) + par) * QP + 512 + p * 8;
        av[1] = (2 * min(16 * qb + 16 + (ln >> 2), lim) + par) * QP + 512 + p * 8;
        const int out = (int)(~((0x1feu << fr) & (s0 + qb == 0 ? ~0xffu : ~0u)) >> (4 * f4));
        unsigned b0[4], b1[4];
#pragma unroll
        for (int i = 0; i < 4; ++i) {
            b0[i] = (unsigned)__builtin_amdgcn_sbfe(out, i, 1); b1[i] = (unsigned)__builtin_amdgcn_sbfe(out, 16 + i, 1);
            c0[i] = __uint_as_float(b0[i] & 0xf149f2cau);
            c1[i] = __uint_as_float(b1[i] & 0xf149f2cau);
        }
#pragma unroll
        for (int i = 0; i < 2; ++i) {
            m0[i] = ~((b0[2 * i] & 0xffffu) | (b0[2 * i + 1] & 0xffff0000u));
            m1[i] = ~((b1[2 * i] & 0xffffu) | (b1[2 * i + 1] & 0xffff0000u));
        }
        az[0] = av[0] + (p == 3 ? 32 - 256 : 32); az[1] = av[1] + (p == 3 ? 32 - 256 : 32);
        og = (unsigned)((rt * 8 + w) * 16 + hg * 4) * 1024u + ln * 16u;
        apad = ((w >> 2) * 80 + c * 8 + (ln >> 3)) * QP + (ln & 7) * 64 + 56;
        wt = ((WM ? rt * 4 + hg : qi * 8 + w) * 49 + min(ln, 48u)) * 128;
        wv = wo_off(ln, w * 8);
        asm volatile("" : "+v"(aq), "+v"(ak[0]), "+v"(ak[1]), "+v"(av[0]), "+v"(av[1]), "+v"(c0), "+v"(c1), "+v"(m0[0]), "+v"(m0[1]), "+v"(m1[0]), "+v"(m1[1]),
                          "+v"(az[0]), "+v"(az[1]), "+v"(og), "+v"(apad), "+v"(wt), "+v"(wv));
    };
#define FTA(i)
    auto wreq = [&](int t) {
        if (t <= -10) { FTA(-10 - t); }
        else if (t == -6) {
            asm volatile("s_mov_b32 m0, %1\n\ts_nop 0\n\tglobal_load_lds_dword %0, off" ::"v"(tc + (u64)(long)((int)(tf & 7u) * 128 - 1024)), "i"(Q_DUMMY) : "memory");
            tf >>= 4;
        }
        else if (t == -7) asm volatile("s_mov_b32 m0, %1\n\ts_nop 0\n\tglobal_load_lds_dword %0, off" ::"v"(tc - 1024 + ((tk + T0B * 128u) & 0x380u)), "i"(Q_DUMMY) : "memory");
        else if (t == -5) {
            asm volatile("s_mov_b32 m0, %2\n\ts_nop 0\n\tglobal_load_lds_dword %0, %1" ::"v"(max(wd, 0)), "s"(wo), "i"(Q_DUMMY) : "memory");
            wd += WL * 4 * 128;
        } else if (t < -1) {
            asm volatile("s_mov_b32 m0, %1\n\ts_nop 0\n\tglobal_load_lds_dword %0, off" ::"v"(tc + (tk & 0x380u)), "i"(Q_DUMMY) : "memory");
            tk += 256u;
        }
        else if (t < 0 && WM && w != WM) {}
        else if (t < 0) asm volatile("s_mov_b32 m0, %2\n\ts_nop 0\n\tglobal_load_lds_dword %0, %1" ::"v"(wt), "s"(wo), "i"(Q_DUMMY) : "memory");
        else glds16a(woc + t * (64 * 896), wv, smem + W_A + t * 8192 + w * 1024);
    };
    unsigned hv3 = 0;
    if (w == 0) hv3 = qkv_wave<5, 6, 6, false>(smem, xA, wqkv, w, lane, 0, 0, hs, kst, offW, offX, apad, prep, setup, wreq);
    else if (w == 1) qkv_wave<5, 5, 1, false>(smem, xA, wqkv, w, lane, 6, 0, hs, kst, offW, offX, apad, prep, setup, wreq);
    else if (w < 4) qkv_wave<5, 5, 0, false>(smem, xA, wqkv, w, lane, 1 + 5 * w, 0, hs, kst, offW, offX, apad, prep, setup, wreq);
    else qkv_wave<4, 5, 0, true>(smem, xA, wqkv, w, lane, 5 * (w - 4), 5, hs, kst, offW, offX, apad, prep, setup, wreq);
    STAMP(0, 3); STAMPC(0, 4);
    if (w >= 4) {
#pragma unroll
        for (int i = 0; i < (PT >> 24 & 15); ++i) wreq(-2);
    }
    FTA(8);
    if (TW == 3) wreq(-1);
    FTA(9);
    const char* qa = smem + aq;
    const char* ka[2] = {smem + ak[0], smem + ak[1]};
    const char* va[2] = {smem + av[0], smem + av[1]};
    const float scale = 0.18898223650461361f, log2e = 1.4426950408889634f;
    volatile LDS_AS unsigned* cnt = (volatile LDS_AS unsigned*)(smem + L_GO);
    lds_add(cnt + 5 + (w >> 2));
    if (w < 4) __builtin_amdgcn_s_setprio(EP);
    else lds_spin(cnt + 6, 4u);
    lds_spin(cnt + 5, 4u);
    if (TW == 4) wreq(-1);
    FTA(0);
    bf16x8 qf[4], k0[4], k1[4], v0[4], v1[4];
#pragma unroll
    for (int hh = 0; hh < 4; ++hh) {
        qf[hh] = lds_rd128(qa + hh * 64);
        k0[hh] = lds_rd128(ka[0] + hh * 64); k1[hh] = lds_rd128(ka[1] + hh * 64);
    }
    auto rdv = [&](int h0, int h1) {
        SB();
#pragma unroll
        for (int hh = h0; hh < h1; ++hh) {
            v0[hh] = cat(lds_tr(va[0] + hh * 64), lds_tr(va[1] + hh * 64));
            v1[hh] = cat(lds_tr(smem + az[0] + hh * 64), lds_tr(smem + az[1] + hh * 64));
        }
        SB();
    };
    if (VL == 0) rdv(0, 4);
    if (TW == 6) wreq(-1);
    SB();
    FTA(1);
    float l[2][4], mx[2];
#pragma unroll
    for (int hp = 0; hp < 2; ++hp) {
        const f32x4 a0 = MFMA(k0[2 * hp], qf[2 * hp], c0), a1 = MFMA(k1[2 * hp], qf[2 * hp], c1);
        const f32x4 b0 = MFMA(k0[2 * hp + 1], qf[2 * hp + 1], c0), b1 = MFMA(k1[2 * hp + 1], qf[2 * hp + 1], c1);
        if (VL == 1) rdv(2 * hp, 2 * hp + 2);
        if (VL == 2 && hp == 1) rdv(0, 4);
        float t[4];
#pragma unroll
        for (int i = 0; i < 4; ++i) {
            auto r = __builtin_amdgcn_permlane32_swap(__float_as_uint(vmax(a0[i], a1[i])), __float_as_uint(vmax(b0[i], b1[i])), false, false);
            t[i] = vmax(__uint_as_float(r[0]), __uint_as_float(r[1]));
        }
#pragma unroll
        for (int i = 0; i < 2; ++i) {
            unsigned a = pk2(t[2 * i], t[2 * i + 1]);
            a = pk2(__uint_as_float(a << 16) * scale, __uint_as_float(a & 0xffff0000u) * scale);
            l[hp][2 * i] = __uint_as_float(a << 16);
            l[hp][2 * i + 1] = __uint_as_float(a & 0xffff0000u);
        }
        mx[hp] = vmax(vmax3(l[hp][0], l[hp][1], l[hp][2]), l[hp][3]);
    }
    {
        auto r = __builtin_amdgcn_permlane16_swap(__float_as_uint(mx[0]), __float_as_uint(mx[1]), false, false);
        const float c = vmax(__uint_as_float(r[0]), __uint_as_float(r[1])) * -log2e;
        auto q2 = __builtin_amdgcn_permlane16_swap(__float_as_uint(c), __float_as_uint(c), false, false);
        mx[0] = __uint_as_float(q2[0]); mx[1] = __uint_as_float(q2[1]);
    }
    float sm[2];
#pragma unroll
    for (int hp = 0; hp < 2; ++hp) {
#pragma unroll
        for (int i = 0; i < 4; ++i) l[hp][i] = __builtin_amdgcn_exp2f(__builtin_fmaf(l[hp][i], log2e, mx[hp]));
        sm[hp] = (l[hp][0] + l[hp][1]) + (l[hp][2] + l[hp][3]);
    }
    {
        auto r = __builtin_amdgcn_permlane16_swap(__float_as_uint(sm[0]), __float_as_uint(sm[1]), false, false);
        const float c = __builtin_amdgcn_rcpf(__uint_as_float(r[0]) + __uint_as_float(r[1]));
        auto q2 = __builtin_amdgcn_permlane16_swap(__float_as_uint(c), __float_as_uint(c), false, false);
        sm[0] = __uint_as_float(q2[0]); sm[1] = __uint_as_float(q2[1]);
    }
    FTA(2);
    const f32x4 z = {0.f, 0.f, 0.f, 0.f};
    const __amdgpu_buffer_rsrc_t rs_o = __builtin_amdgcn_make_buffer_rsrc(o, 0, 2048 * 512 * 2, 0x00020000);
#pragma unroll
    for (int hp = 0; hp < 2; ++hp) {
        const unsigned p01 = pk2(l[hp][0] * sm[hp], l[hp][1] * sm[hp]), p23 = pk2(l[hp][2] * sm[hp], l[hp][3] * sm[hp]);
        auto e01 = __builtin_amdgcn_permlane32_swap(p01, p01, false, false);
        auto e23 = __builtin_amdgcn_permlane32_swap(p23, p23, false, false);
#pragma unroll
        for (int e = 0; e < 2; ++e) {
            const int hh = 2 * hp + e;
            u32x4 pw = {e01[e] & m0[0], e23[e] & m0[1], e01[e] & m1[0], e23[e] & m1[1]};
            const bf16x8 pf = __builtin_bit_cast(bf16x8, pw);
            const f32x4 o0 = MFMA(v0[hh], pf, z), o1 = MFMA(v1[hh], pf, z);
            st16_sc1(rs_o, og + hh * 1024u, u32x4{pk2(o0[0], o0[1]), pk2(o0[2], o0[3]), pk2(o1[0], o1[1]), pk2(o1[2], o1[3])});
        }
    }
    FTA(4);
    if (w >= 6) lds_add(cnt + 7);
    {
        const Hs me = {hs.base, (unsigned)(HS_O + rt * 256 + (w >> 2) * 64 + hg * 16 + (w & 3) * 4), 0u};
        wait_vm<0>();
        hs_add(me, 1ull);
    }
    FTA(5);
    if (w >= 4) {
#pragma unroll 1
        for (int t = 12; t > 0; --t) glds16a(woc + t * (32 * 896), wv, smem + W_A + t * 4096 + w * 1024);
        lds_spin(cnt + 7, 2u);
        glds16a(woc - 32 * 896, wv, smem + W_A - 4096 + w * 1024);
        glds16a(woc, wv, smem + W_A + w * 1024);
        wait_vm<0>();
        lds_add(cnt + 4);
        END();
    }
    return hv3;
}
#define PA 10
#define NI 8
#define PD3 1
#define KRO 1
#define LR 2
template <class F>
__device__ __forceinline__ void oproj(char* smem, int wb, int so, u16* __restrict__ o, u16* __restrict__ out, int rt, int rg, int ct, int lane, F wait
    ) {
    const int fr = lane & 15, fq = lane >> 4;
    const int q = (lane >> 2) & 3, p = lane & 3, s = (2 * fq + (q >> 1)) & 3;
    const int rot = KRO ? (ct & 1) * 8 : 0;
    unsigned aw[2][4];
#pragma unroll
    for (int c = 0; c < 4; ++c) {
        aw[0][c] = wb + rot * 3584 + (4 * fq + q) * 128 + ((c ^ s) * 32) + 8 * p;
        aw[1][c] = aw[0][c] + (fq < 3 ? 2048 : 0);
    }
    bf16x8 fb[16][4];
    auto rdw = [&](int h) {
        if (KRO && h == 8) {
#pragma unroll
            for (int c = 0; c < 4; ++c) { aw[0][c] -= rot * 7168; aw[1][c] -= rot * 7168; }
        }
#pragma unroll
        for (int c = 0; c < 4; ++c) fb[h][c] = cat(lds_tr(smem + aw[0][c ^ (h & 1) * 2] + h * 3584), lds_tr(smem + aw[1][c ^ (h & 1) * 2] + h * 3584));
    };
    __amdgpu_buffer_rsrc_t rs_o = __builtin_amdgcn_make_buffer_rsrc(o, 0, 2048 * 512 * 2, 0x00020000);
    __amdgpu_buffer_rsrc_t rs_out = __builtin_amdgcn_make_buffer_rsrc(out, 0, 2048 * 896 * 2, 0x00020000);
    unsigned ga = (unsigned)(rt * 8 + rg) * 16384u + (unsigned)lane * 16u;
    unsigned sw = so + fr * O_P + fq * 8, sr = so + (lane >> 3) * O_P + (lane & 7) * 16;
    unsigned ob = (unsigned)((rt * 128 + (rg >> 1) * 32 + 2 * (lane >> 3) + (rg & 1)) * 1792 + ct * 128 + (lane & 7) * 16);
    f32x4 acc[4];
#pragma unroll
    for (int c = 0; c < 4; ++c) { acc[c] = f32x4{0.f, 0.f, 0.f, 0.f}; asm volatile("" : "+v"(acc[c]), "+v"(aw[0][c]), "+v"(aw[1][c])); }
    asm volatile("" : "+v"(ob), "+v"(sw), "+v"(sr), "+v"(ga));
    int so4[4];
#pragma unroll
    for (int i = 0; i < 4; ++i) so4[i] = (i << 12) ^ rot << 10;
    asm volatile("" : "+s"(rs_o), "+s"(rs_out), "+s"(so4[0]), "+s"(so4[1]), "+s"(so4[2]), "+s"(so4[3]));
    wait(0);
    bf16x8 fa[16];
    auto lda = [&](int ks) { fa[ks] = __builtin_bit_cast(bf16x8, __builtin_amdgcn_raw_buffer_load_b128(rs_o, ga + (ks & 3) * 1024u, so4[ks >> 2], 16)); };
#pragma unroll
    for (int ks = 0; ks < NI; ++ks) lda(ks);
    SB(); QT(4);
    wait(1);
#pragma unroll
    for (int ks = 0; ks < PA; ++ks) rdw(ks);
    SB(); QT(5);
#pragma unroll
    for (int ks = 0; ks < 16; ++ks) {
#pragma unroll
        for (int c = 0; c < 4; ++c) {
            acc[c] = MFMA(fb[ks][c], fa[ks], acc[c]);
            if (c < LR && ks * LR + c + NI < 16) { SB(); lda(ks * LR + c + NI); SB(); }
        }
        SB();
        if (ks + PA < 16) rdw(ks + PA);
        if (ks == 0) QT(6);
        if (ks == 4) QT(7);
        if (ks == 9) QT(8);
        if (ks == 15) QT(9);
        SB();
    }
    STAMP(1, 3);
#pragma unroll
    for (int c = 0; c < 4; ++c) *reinterpret_cast<u32x2*>(smem + sw + c * 32) = u32x2{pk2(acc[c][0], acc[c][1]), pk2(acc[c][2], acc[c][3])};
    asm volatile("" ::: "memory");
    const u32x4 r0 = *reinterpret_cast<const u32x4*>(smem + sr), r1 = *reinterpret_cast<const u32x4*>(smem + sr + 8 * O_P);
    SB();
    wait(2);
    __builtin_amdgcn_raw_buffer_store_b128(r0, rs_out, ob, 0, OPOL);
    __builtin_amdgcn_raw_buffer_store_b128(r1, rs_out, ob, 16 * 1792, OPOL);
}

__global__ __launch_bounds__(512) void kf(const u16* xA, const u16* wqkv, const char* xB, const char* Hr, const char* Hi, const char* Cr,
                                          const char* Ci, const char* wo, u16* out, char* ws) {
    __shared__ __attribute__((aligned(16))) char smem[LDS_TOTAL];
    int tid = threadIdx.x, bid = blockIdx.x;
    u64 pc0;
    {
        unsigned part, off;
        asm volatile("s_getpc_b64 %0\n\ts_bfe_u32 %1, %2, 0x30003\n\ts_mulk_i32 %1, %3" : "=&s"(pc0), "=&s"(part), "+s"(bid) : "i"(CODE_PART) : "scc");
        asm volatile("s_mov_b32 m0, %4\n\tv_mad_u32_u24 %0, %1, 12, %2\n\tglobal_load_lds_dword %0, %3" : "=&v"(off), "+v"(tid) : "s"(part), "s"(pc0), "i"(Q_DUMMY) : "memory");
        asm volatile("" : "+s"(bid));
    }
    __builtin_assume(tid >= 0 && tid < 512 && bid >= 0 && bid < 256);
    const int lane = tid & 63, xcd = bid & 7;
    int w = __builtin_amdgcn_readfirstlane(tid >> 6), j = bid >> 3;
    STAMP(0, 0);
    const int ks = xcd & 3, rl = j & 3, c6 = j >> 2, r8 = (xcd >> 2) * 4 + rl;
    BL q;
    if (j < 24 && w < 4) {
        unsigned ox = (unsigned)(((ks & 1) * 1024 + r8 * 64) * 896), oh = (unsigned)((c6 < 5 ? c6 * 80 : 384) * 896);
#define KR 1
        q.kc = c6 + KR * rl >= 7 ? c6 + KR * rl - 7 : c6 + KR * rl;
        b_off(q, c6 < 4 ? 79 : 63, lane, w);
        const char *p0 = xB, *p1 = Hr, *p2 = Hi;
        asm volatile("" : "+v"(q.offX), "+v"(q.offH[0]), "+v"(q.offH[1]), "+v"(q.offH[2]));
        asm volatile("" : "+s"(p0), "+s"(p1), "+s"(p2), "+s"(ox), "+s"(oh), "+s"(q.kc));
        q.xg = p0 + ox;
        q.hg = ((ks >> 1) ? p2 : p1) + oh;
        STAMP(0, 7);
        reqH(q, smem, w, 0); reqX(q, smem, w);
        reqH(q, smem, w, 1); reqX(q, smem, w);
        asm volatile("" ::"s"(xA), "s"(wqkv), "s"(Cr), "s"(Ci), "s"(wo), "s"(out), "s"(ws));
    }
    asm volatile("" : "+s"(w), "+s"(j));
    __builtin_assume(w >= 0 && w < 8 && j >= 0 && j < 32);
    if (w == 7) *reinterpret_cast<unsigned*>(smem + L_GO + (lane & 7) * 4) = 0u;
    Hs hs;
    hs.base = (const unsigned*)(ws + WS_HS); hs.off = 0; hs.snap = 0;
    int ro = 0, rg = 0, ct = 0;
    bool duty = true;
    u64 again = 0;
    unsigned ka = 0, kb = 0;
    int wb = W_0;
    unsigned full = 4u;
    if (j < 24) {
        u16* ab = (u16*)ws;
        u16* px = (u16*)(ws + WS_PX);
        const int rbl = j & 7, cg = j >> 3, rb = (xcd >> 2) * 8 + rbl;
#define DU 1
        const int un = DU ? j - min(c6, 4) : j - 4;
        ro = xcd * 2 + (un >> 1 & 1); ct = un >> 2; rg = (un & 1) * 4 + (w & 3);
        const int g1 = ks * 3 + cg;
        if (w < 4) {
            const char* cgp = ((ks >> 1) ? Ci : Cr) + (long)rb * (128 * 4096) + (ks & 1) * 2048;
            b_load(smem, q, cgp, (const char*)ab + g1 * (16 * B_SL), (u64)(((cg == 0 ? TB0 : cg == 1 ? TB1 : TB2) + rbl * 0x11111111u) & 0x77777777u) << 32 |
#define OWN 0
                   early(((cg == 0 ? TC0 : cg == 1 ? TC1 : TC2) + (RA >> 4 * rbl & 7) * 0x11111111u) & 0x77777777u), ks, cg == 2,
                   wo + ct * 128, (j & 19) != 3 ? M_OR : 0ull, hs, lane, w, r8 * 2 + (c6 & 1)
                   );
        } else {
            const int cw = w - 4;
            asm volatile("" ::"s"(xB), "s"(Hr), "s"(Hi));
            hs.off = hs_off((lane & 56) == 32 ? (lane & 60) | ((ks - lane) & 3) : lane, g1 * 256, HS_X + ((rb * 3 + cg) * 4 + cw) * 256, ro, ct * 2 + (un & 1));
            unsigned v1 = 0;
            hs_rmw(hs, v1, w == 5 ? M_LO | M_HI | M_XW | M_XR | M_OW : M_XW | M_XR);
            STAMP(0, 7);
            auto post = [&]() {
                if (w == 5) *reinterpret_cast<u32x2*>(smem + L_KEY + lane * 8) = u32x2{hs.off, hs_take<0>(v1)};
            };
            const unsigned dbase = (unsigned)((g1 * 16 + r8 * 2) * B_SL + (c6 & 1) * (c6 < 4 ? 80 : 64) * 64);
            const unsigned sg = (ks >> 1) ? 0x80008000u : 0u;
            if (c6 < 4 && !(cw & 1)) ab_mma<3>(smem, dbase, sg, lane, cw >> 1, 0, ab, post);
            else ab_mma<2>(smem, dbase, sg, lane, cw >> 1, (cw & 1) * (c6 < 4 ? 3 : 2), ab, post);
            hs.snap = hs_take<63>(v1);
            hs_add(hs, 1ull << 36);
            const unsigned pxb = (unsigned)(rb * 3 + cg) * 131072u, ob0 = (unsigned)((rb * 128 + ks * 16) * 896 + 448 + cg * 160) * 2u;
            if (cg == 2) y_mma<4>(smem, lane, cw, px, pxb, ks, out, ob0, hs);
            else y_mma<5>(smem, lane, cw, px, pxb, ks, out, ob0, hs);
            duty = false;
        }
    } else {
        const int qi = j - 24;
        if (AD) __builtin_amdgcn_s_sleep(AD);
#define AM 1
        const int hg = xcd & 3, rt = (xcd >> 2) * 8 + qi;
#define XT4 0x5401
        int kst = qi + (XT4 >> (4 * hg) & 15); kst = kst >= 7 ? kst - 7 : kst;
        ro = xcd * 2 + (qi >> 1 & 1); ct = 5 + (qi >> 2); rg = (qi & 1) * 4 + (w & 3); duty = w < 4;
        const unsigned hv = qkv_attn(smem, xA, wqkv, wo, ws, rt, hg, tid, w, hs, kst, ro, ct * 2 + (qi & 1),
                                     (u64)(((ks >> 1) ? Ci : Cr) + (long)rt * (128 * 4096) + (ks & 1) * 2048 + 1024), qi, wo + ct * 128);
        STAMP(0, 21);
        if (w == 0 && !hs_new(hs, hv, M_R14)) again = 0xf000full << 4 * hg;
        wb = W_A; full = 8u;
    }
    if (duty) {
        QDECL;
        QT(0);
        if (w == 0) hs_rmw(hs, ka, M_OW);
        if (j < 24) {
            const unsigned offW = wo_off(lane, w * 32);
#pragma unroll
            for (int n = WP2; n < 14; ++n) wo_req(smem, wo + ct * 128, offW, w, n);
        }
        SB();
        auto wait = [&](int ph) {
            volatile LDS_AS unsigned* go = (volatile LDS_AS unsigned*)(smem + L_GO);
            if (ph == 0) {
                if (w == 0) { __builtin_amdgcn_s_sleep(PD3); hs_wait2(hs, M_OW, ka, kb); lds_add(go); } else wait_vm<0>();
                lds_add(go + 4);
                lds_spin<GSL>(go, 9u - full);
                STAMP(1, 0); QT(1);
            } else if (ph == 1) {
                if (w == 0) hs_add(hs, M_OR);
                lds_spin(go + 4, full);
            } else if (GT && j < 24) lds_spin<GSL>(go + 5, GT);
        };
        oproj(smem, wb, O_ST + (w & 3) * (16 * O_P), (u16*)(ws + WS_O), out, ro, rg, ct, lane, wait
              );
        QT(2);
        asm volatile("s_cmp_eq_u64 %0, 0\n\ts_cbranch_scc0 1f\n\ts_endpgm\n1:" ::"s"(again), "v"(ka), "v"(kb) : "memory", "scc");
        hs_keep(ka, kb);
        QT(3); QOUT(10);
        if (again) { hs_wait(hs, M_R14, M_R14, 0u); hs_add(hs, again); }
    }
    STAMP(1, 24);
}

extern "C" void kernel_launch(void* const* d_in, const int* in_sizes, int n_in, void* d_out, int out_size,
                              void* d_ws, size_t ws_size, hipStream_t stream) {
    kf<<<256, 512, 0, stream>>>((const u16*)d_in[0], (const u16*)d_in[1], (const char*)d_in[3], (const char*)d_in[6], (const char*)d_in[7],
                                (const char*)d_in[4], (const char*)d_in[5], (const char*)d_in[2], (u16*)d_out, (char*)d_ws);
}
```
